```python
import torch

def _softmax(x, axis=-1):
    return torch.softmax(x, dim=axis)
def _gelu(x):
    return torch.nn.functional.gelu(x, approximate="tanh")
def _silu(x):
    return torch.nn.functional.silu(x)
def _sigmoid(x):
    return torch.sigmoid(x)
def _layernorm(x, axis=-1, eps=1e-5):
    m = x.mean(dim=axis, keepdim=True)
    v = x.var(dim=axis, keepdim=True, unbiased=False)
    return (x - m) / torch.sqrt(v + eps)
def _scatter_add(tgt, idx, src, axis=0):
    return tgt.clone().index_add_(axis, idx, src)
def _segment_sum(src, seg, n):
    out = torch.zeros((n,) + src.shape[1:], dtype=src.dtype)
    return out.index_add_(0, seg, src)
def _pad(x, pw):
    flat = tuple(v for pair in reversed(pw) for v in pair)
    return torch.nn.functional.pad(x, flat)

kernel_name = 'recurrent_scan__softmax_full_mha__mixer_channel_mlp__prompt'

def setup_inputs():

    _w = (lambda a: (lambda t: t.to(torch.bfloat16) if t.is_floating_point() else t)(torch.as_tensor(a)))
    def _A_setup():
        B, S, D = 1, 2048, 640
        return {
            "x": _w((lambda *s: torch.randn(*s, dtype=torch.bfloat16) * 0.05)(B, S, D)),
            "wqkv": _w((lambda *s: torch.randn(*s, dtype=torch.bfloat16) * 0.05)(D, 3 * D)),
            "wo": _w((lambda *s: torch.randn(*s, dtype=torch.bfloat16) * 0.05)(D, D)),
        }
    _A_in = _A_setup()

    def _B_setup():
        B, S, D, F = 1, 2048, 640, 1728
        return {
            "x": _w((lambda *s: torch.randn(*s, dtype=torch.bfloat16) * 0.05)(B, S, D)),
            "w1": _w((lambda *s: torch.randn(*s, dtype=torch.bfloat16) * 3.2)(D, F) * (D ** -0.5)),
            "b1": _w((lambda *s: torch.randn(*s, dtype=torch.bfloat16) * 1.6)(F)),
            "w2": _w((lambda *s: torch.randn(*s, dtype=torch.bfloat16) * 0.05)(F, D) * (F ** -0.5)),
            "b2": _w((lambda *s: torch.randn(*s, dtype=torch.bfloat16) * 0.05)(D)),
        }
    _B_in = _B_setup()

    return {
        "A_x": _A_in["x"],
        "A_wqkv": _A_in["wqkv"],
        "A_wo": _A_in["wo"],
        "B_x": _B_in["x"],
        "B_w1": _B_in["w1"],
        "B_b1": _B_in["b1"],
        "B_w2": _B_in["w2"],
        "B_b2": _B_in["b2"],
    }

def reference(A_x, A_wqkv, A_wo, B_x, B_w1, B_b1, B_w2, B_b2):
    def _A_ref(x, wqkv, wo):
        B, S, D = x.shape
        H = 32; Dh = D // H
        qkv = (lambda x, *d: x.permute(*d))((x @ wqkv).reshape(B, S, 3, H, Dh), 2, 0, 3, 1, 4)
        q, k, v = qkv[0], qkv[1], qkv[2]
        scale = Dh ** -0.5
        scores = torch.einsum("bhqd,bhkd->bhqk", q, k) * scale
        p = _softmax(scores, axis=-1)
        o = torch.einsum("bhqk,bhkd->bhqd", p, v)
        return (lambda x, *d: x.permute(*d))(o, 0, 2, 1, 3).reshape(B, S, D) @ wo

    def _B_ref(x, w1, b1, w2, b2):
        y = _layernorm(x, axis=-1)
        h = _gelu(torch.einsum("bsd,df->bsf", y, w1) + b1)
        return torch.einsum("bsf,fd->bsd", h, w2) + b2

    x = A_x
    xn = _layernorm(x, axis=-1)
    s = _A_ref(xn, A_wqkv, A_wo)
    g = _silu(_B_ref(xn, B_w1, B_b1, B_w2, B_b2))
    return x + s * g
```

```cpp
#include <hip/hip_runtime.h>
#include <hip/hip_bf16.h>
#include <stdint.h>
#include <type_traits>

typedef short bf16x8 __attribute__((ext_vector_type(8)));
typedef short bf16x4 __attribute__((ext_vector_type(4)));
typedef float f32x16 __attribute__((ext_vector_type(16)));
typedef float f32x4 __attribute__((ext_vector_type(4)));
typedef unsigned u32x4 __attribute__((ext_vector_type(4)));
typedef unsigned u32x2 __attribute__((ext_vector_type(2)));
typedef __bf16 bf2_t __attribute__((ext_vector_type(2)));
typedef short v4i16_t __attribute__((ext_vector_type(4)));
typedef unsigned short u16;
#define LDS_AS __attribute__((address_space(3)))
typedef LDS_AS const char* lds_cptr;
typedef LDS_AS char* lds_ptr;
#define DI __device__ __forceinline__

constexpr int SEQ = 2048, DM = 640, NH = 32, DH = 20, FF = 1728;
constexpr float LN_EPS = 1e-5f;
constexpr float LOG2E = 1.4426950408889634f;
constexpr float QSCALE = 0.22360679774997896f * LOG2E;

constexpr int QT_B = 1280, KVT_B = 3072;
constexpr size_t WS_Q = 0;
constexpr size_t WS_KV = WS_Q + (size_t)NH * 64 * QT_B;
constexpr size_t WS_H = WS_KV + (size_t)NH * 64 * KVT_B;
constexpr size_t WS_O = WS_H + (size_t)64 * 108 * 1024;
constexpr size_t WS_G = WS_O + (size_t)64 * 40 * 1024;

#define MFMA32(a, b, c) __builtin_amdgcn_mfma_f32_32x32x16_bf16((a), (b), (c), 0, 0, 0)
#define MFMA32K8(a, b, c) __builtin_amdgcn_mfma_f32_32x32x8bf16_1k((a), (b), (c), 0, 0, 0)

typedef float f32x2 __attribute__((ext_vector_type(2)));
DI unsigned cvtpk(float lo, float hi) { return __builtin_bit_cast(unsigned, __builtin_convertvector((f32x2){lo, hi}, bf2_t)); }
DI float bf_lo(unsigned u) { return __uint_as_float(u << 16); }
DI float bf_hi(unsigned u) { return __uint_as_float(u & 0xffff0000u); }
DI float dot2(unsigned a, unsigned b, float c) { return __builtin_amdgcn_fdot2_f32_bf16(__builtin_bit_cast(bf2_t, a), __builtin_bit_cast(bf2_t, b), c, false); }
DI void glds16(const void* g, unsigned lds_base) {
    unsigned sv;
    asm volatile("s_mov_b32 %0, m0\n\ts_mov_b32 m0, %2\n\ts_nop 0\n\tglobal_load_lds_dwordx4 %1, off\n\ts_mov_b32 m0, %0"
                 : "=&s"(sv) : "v"(g), "s"(lds_base) : "memory");
}
DI bf16x8 lds128(lds_cptr p) { return *(const LDS_AS bf16x8*)p; }
DI bf16x4 lds64(lds_cptr p) { return *(const LDS_AS bf16x4*)p; }
DI bf16x4 ldstr(lds_cptr p) { return __builtin_bit_cast(bf16x4, __builtin_amdgcn_ds_read_tr16_b64_v4i16((LDS_AS v4i16_t*)p)); }
DI bf16x8 cat8(bf16x4 a, bf16x4 b) { return __builtin_shufflevector(a, b, 0, 1, 2, 3, 4, 5, 6, 7); }
DI float xhalf(float v) {
    auto r = __builtin_amdgcn_permlane32_swap(__float_as_uint(v), __float_as_uint(v), false, false);
    return (threadIdx.x & 32) ? __uint_as_float(r[0]) : __uint_as_float(r[1]);
}
#define STAMP(ws, kid, i)
#define WAITV(n) asm volatile("s_waitcnt vmcnt(" #n ")" ::: "memory")
#define WAITV_BAR(n) asm volatile("s_waitcnt vmcnt(" #n ") lgkmcnt(0)\n\ts_barrier" ::: "memory")
#define BAR_L() asm volatile("s_waitcnt lgkmcnt(0)\n\ts_barrier" ::: "memory")

constexpr int K1_ASZ = 256 * 128, K1_BSZ = 64 * 256, K1_STG = K1_ASZ + K1_BSZ, K1_NST = 3;
constexpr int K1_LDS = K1_NST * K1_STG;

DI float gelu_tanh(float u) {
    const float c1 = -2.0f * 0.7978845608028654f * LOG2E, c2 = c1 * 0.044715f;
    const float t = u * __builtin_fmaf(u * u, c2, c1);
    return u * __builtin_amdgcn_rcpf(1.0f + __builtin_amdgcn_exp2f(t));
}

__global__ __launch_bounds__(512) void k1_proj(const u16* __restrict__ x, const u16* __restrict__ wqkv, const u16* __restrict__ w1,
                                               const u16* __restrict__ b1, char* __restrict__ ws) {
    extern __shared__ __attribute__((aligned(16))) char smem[];
    const int tid = threadIdx.x, lane = tid & 63, wid = __builtin_amdgcn_readfirstlane(tid >> 6);
    const int g8 = blockIdx.x & 7, idx = blockIdx.x >> 3;
    const int nq = g8 >> 1, pi = idx >> 2;
    if (pi >= (nq == 3 ? 8 : 7)) return;
    const int mt = (g8 & 1) * 4 + (idx & 3), panel = nq * 7 + pi;
    const int m0 = mt * 256;
    STAMP(ws, 0, 0);
    const bool is_mlp = panel >= 15;
    const u16* W = is_mlp ? w1 : wqkv;
    const int ldw = is_mlp ? FF : 3 * DM;
    const int n0 = (is_mlp ? panel - 15 : panel) * 128;
    const bool half_panel = panel == 28;
    const int arow = wid * 32 + (lane >> 3);
    const u16* asrc = x + (size_t)(m0 + arow) * DM + (((lane & 7) ^ ((arow >> 1) & 7)) << 3);
    const int brow = wid * 8 + (lane >> 4);
    int bch = (lane & 15) ^ ((brow & 3) << 2);
    if (half_panel) bch &= 7;
    const u16* bsrc = W + (size_t)brow * ldw + n0 + bch * 8;
    const unsigned lds0 = (unsigned)(uintptr_t)smem;
    const lds_cptr lds = (lds_cptr)smem;

    f32x16 acc[2][2];
#pragma unroll
    for (int i = 0; i < 2; ++i)
#pragma unroll
        for (int j = 0; j < 2; ++j)
#pragma unroll
            for (int e = 0; e < 16; ++e) acc[i][j][e] = 0.f;
    float rs = 0.f, rq = 0.f, cs = 0.f;
    const bool is_v = panel >= 10 && panel < 15;
    const int wm = wid >> 1, wn = wid & 1, hh = lane >> 5, r = lane & 31;

    const u16* asrc_e = asrc;
    const u16* asrc_o = x + (size_t)(m0 + arow) * DM + ((((lane & 7) ^ ((arow >> 1) & 7)) ^ 4) << 3);
    auto run = [&](auto TR) {
        constexpr bool TRANS = decltype(TR)::value;
        const int par = wm >> 1;
        int aoff[4][2];
#pragma unroll
        for (int kk = 0; kk < 4; ++kk)
#pragma unroll
            for (int mi = 0; mi < 2; ++mi)
                aoff[kk][mi] = (wm * 64 + (mi ^ wn) * 32 + r) * 128 + (((2 * (kk ^ par) + hh) ^ ((r >> 1) & 7)) << 4);
        const int g = lane >> 4, q = (lane & 15) >> 2, p = lane & 3;
        int boff[2][2];
#pragma unroll
        for (int ni = 0; ni < 2; ++ni) {
            const int c = wn * 8 + (ni ^ (wm & 1)) * 4 + 2 * (g & 1) + (p >> 1);
#pragma unroll
            for (int k1 = 0; k1 < 2; ++k1)
                boff[ni][k1] = K1_ASZ + (k1 ^ par) * 4096 + (8 * (g >> 1) + q) * 256 + ((c ^ (q << 2)) << 4) + (p & 1) * 8;
        }
        auto issue = [&](int st, int slot) {
            const unsigned dst = lds0 + slot * K1_STG;
#pragma unroll
            for (int i = 0; i < 4; ++i) glds16(((i & 1) ? asrc_o : asrc_e) + (size_t)i * 8 * DM + st * 64, dst + (wid * 4 + i) * 1024);
#pragma unroll
            for (int i = 0; i < 2; ++i) glds16(bsrc + (size_t)(st * 64 + i * 4) * ldw, dst + K1_ASZ + (wid * 2 + i) * 1024);
        };
        issue(0, 0);
        issue(1, 1);
        int slot = 0;
        for (int st = 0; st < 10; ++st) {
            if (st < 9) { WAITV_BAR(6); } else { WAITV_BAR(0); }
            if (st == 0) STAMP(ws, 0, 1);
            if (st == 5) STAMP(ws, 0, 2);
            if (st < 8) { const int s2 = slot == 0 ? 2 : slot - 1; issue(st + 2, s2); }
            const lds_cptr sb = lds + slot * K1_STG;
#pragma unroll
            for (int kk = 0; kk < 4; ++kk) {
                bf16x8 af[2], bfr[2];
#pragma unroll
                for (int mi = 0; mi < 2; ++mi) af[mi] = lds128(sb + aoff[kk][mi]);
#pragma unroll
                for (int ni = 0; ni < 2; ++ni) bfr[ni] = cat8(ldstr(sb + boff[ni][kk & 1] + (kk & 2) * 4096), ldstr(sb + boff[ni][kk & 1] + (kk & 2) * 4096 + 1024));
#pragma unroll
                for (int mi = 0; mi < 2; ++mi)
#pragma unroll
                    for (int ni = 0; ni < 2; ++ni)
                        acc[mi][ni] = TRANS ? MFMA32(bfr[ni], af[mi], acc[mi][ni]) : MFMA32(af[mi], bfr[ni], acc[mi][ni]);
                const u32x4 a = __builtin_bit_cast(u32x4, af[0]);
#pragma unroll
                for (int j = 0; j < 4; ++j) { rs = dot2(a[j], 0x3f803f80u, rs); rq = dot2(a[j], a[j], rq); }
                if ((kk & 1) == 0) {
                    const u32x4 b = __builtin_bit_cast(u32x4, bfr[0]);
#pragma unroll
                    for (int j = 0; j < 4; ++j) cs = dot2(b[j], 0x3f803f80u, cs);
                }
            }
            slot = slot == 2 ? 0 : slot + 1;
        }
    };
    if (is_v) run(std::integral_constant<bool, false>{}); else run(std::integral_constant<bool, true>{});

    STAMP(ws, 0, 3);
    BAR_L();
    float* st_mu = (float*)smem;
    float* st_sc = st_mu + 256;
    float* st_cs = st_sc + 256;
    rs += xhalf(rs); rq += xhalf(rq); cs += xhalf(cs);
    if (hh == 0) {
        const float mu = rs * (1.0f / DM);
        const float var = __builtin_fmaxf(rq * (1.0f / DM) - mu * mu, 0.f);
        const float r1 = 1.0f / __builtin_sqrtf(var + LN_EPS);
        float sc = r1;
        if (is_mlp) sc = r1 / __builtin_sqrtf(var * r1 * r1 + LN_EPS);
        else if (panel < 5) sc = r1 * QSCALE;
        st_mu[wm * 64 + wn * 32 + r] = mu;
        st_sc[wm * 64 + wn * 32 + r] = sc;
        st_cs[(wm >> 1) * 128 + wn * 64 + (wm & 1) * 32 + r] = cs;
    }
    BAR_L();
    STAMP(ws, 0, 4);
    if (half_panel && wn == 1) return;

    if (is_v) {
#pragma unroll
        for (int ni = 0; ni < 2; ++ni) {
            const int cl = wn * 64 + (ni ^ (wm & 1)) * 32 + r;
            const float c = st_cs[cl] + st_cs[128 + cl];
            const int nn = n0 - 2 * DM + cl, head = nn / DH, d = nn - head * DH;
#pragma unroll
            for (int mi = 0; mi < 2; ++mi) {
                const int rl = wm * 64 + (mi ^ wn) * 32;
                char* dst = ws + WS_KV + (size_t)(head * 64 + ((m0 + rl) >> 5)) * KVT_B + 1536 + (hh * 24 + d) * 16;
#pragma unroll
                for (int s = 0; s < 2; ++s) {
                    u32x4 o;
#pragma unroll
                    for (int gq = 0; gq < 2; ++gq) {
                        const f32x4 mu = *(const f32x4*)(st_mu + rl + 16 * s + 8 * gq + 4 * hh);
                        const f32x4 sc = *(const f32x4*)(st_sc + rl + 16 * s + 8 * gq + 4 * hh);
                        float v[4];
#pragma unroll
                        for (int e = 0; e < 4; ++e) v[e] = __builtin_fmaf(-mu[e], c, acc[mi][ni][8 * s + 4 * gq + e]) * sc[e];
                        o[2 * gq] = cvtpk(v[0], v[1]); o[2 * gq + 1] = cvtpk(v[2], v[3]);
                    }
                    *(u32x4*)(dst + s * 768) = o;
                    if (d == DH - 1) *(u32x4*)(dst + s * 768 + 16) = (u32x4){0x3f803f80u, 0x3f803f80u, 0x3f803f80u, 0x3f803f80u};
                }
            }
        }
    } else {
#pragma unroll
        for (int mi = 0; mi < 2; ++mi) {
            const int rl = wm * 64 + (mi ^ wn) * 32 + r;
            const float mu = st_mu[rl], sc = st_sc[rl];
            const int tt = (m0 + wm * 64 + (mi ^ wn) * 32) >> 5;
#pragma unroll
            for (int ni = 0; ni < 2; ++ni) {
                unsigned pk[8];
#pragma unroll
                for (int gq = 0; gq < 4; ++gq) {
                    const int cl = wn * 64 + (ni ^ (wm & 1)) * 32 + 8 * gq + 4 * hh;
                    const f32x4 c0 = *(const f32x4*)(st_cs + cl), c1 = *(const f32x4*)(st_cs + 128 + cl);
                    float v[4];
#pragma unroll
                    for (int e = 0; e < 4; ++e) v[e] = __builtin_fmaf(-mu, c0[e] + c1[e], acc[mi][ni][4 * gq + e]) * sc;
                    if (is_mlp) {
                        const u32x2 bb = *(const u32x2*)(b1 + n0 + cl);
                        v[0] = gelu_tanh(v[0] + bf_lo(bb[0])); v[1] = gelu_tanh(v[1] + bf_hi(bb[0]));
                        v[2] = gelu_tanh(v[2] + bf_lo(bb[1])); v[3] = gelu_tanh(v[3] + bf_hi(bb[1]));
                    }
                    pk[2 * gq] = cvtpk(v[0], v[1]); pk[2 * gq + 1] = cvtpk(v[2], v[3]);
                }
                if (is_mlp) {
                    const int ks = (n0 + wn * 64 + (ni ^ (wm & 1)) * 32) >> 4;
                    char* dst = ws + WS_H + ((size_t)tt * 108 + ks) * 1024 + lane * 16;
                    *(u32x4*)dst = (u32x4){pk[0], pk[1], pk[2], pk[3]};
                    *(u32x4*)(dst + 1024) = (u32x4){pk[4], pk[5], pk[6], pk[7]};
                } else {
                    const bool isk = panel >= 5;
#pragma unroll
                    for (int gq = 0; gq < 4; ++gq) {
                        const int nn = n0 - (isk ? DM : 0) + wn * 64 + (ni ^ (wm & 1)) * 32 + 8 * gq + 4 * hh;
                        const int head = nn / DH, pc = (nn - head * DH) >> 2;
                        char* base = isk ? ws + WS_KV + (size_t)(head * 64 + tt) * KVT_B : ws + WS_Q + (size_t)(head * 64 + tt) * QT_B;
                        const int off = pc < 4 ? (pc >> 1) * 512 + r * 16 + (pc & 1) * 8 : 1024 + r * 8;
                        *(u32x2*)(base + off) = (u32x2){pk[2 * gq], pk[2 * gq + 1]};
                        if (isk && pc == 4) *(u32x2*)(base + 1280 + r * 8) = (u32x2){0x3f803f80u, 0x00003f80u};
                    }
                }
            }
        }
    }
    STAMP(ws, 0, 5);
    WAITV(0);
    STAMP(ws, 0, 6);
}

template <int KSW, int RING>
DI void gemm32x160(const char* __restrict__ af, size_t af_off, const u16* __restrict__ W, int n0, char* smem, int wid, int lane, f32x4 (&res)[5]) {
    const unsigned lds0 = (unsigned)(uintptr_t)smem + wid * (RING * 5120);
    const lds_cptr lds = (lds_cptr)smem + wid * (RING * 5120);
    const int ks0 = wid * KSW;
    const u16* bsrc[5];
#pragma unroll
    for (int i = 0; i < 5; ++i) {
        const int o = i * 64 + lane, row = o / 20, ch = o - row * 20;
        bsrc[i] = W + (size_t)(ks0 * 16 + row) * DM + n0 + ch * 8;
    }
    const char* asrc = af + (size_t)ks0 * 1024 + lane * 16;
    const int g = lane >> 4, q = (lane & 15) >> 2, p = lane & 3;
    const int boff = (4 * (g >> 1) + q) * 320 + (16 * (g & 1) + 4 * p) * 2;
    f32x16 acc[5];
#pragma unroll
    for (int u = 0; u < 5; ++u)
#pragma unroll
        for (int e = 0; e < 16; ++e) acc[u][e] = 0.f;
    auto issue = [&](int ks, int slot) {
#pragma unroll
        for (int i = 0; i < 5; ++i) glds16(bsrc[i] + (size_t)ks * 16 * DM, lds0 + slot * 5120 + i * 1024);
    };
    u32x4 areg[RING];
    auto aload = [&](int ks, u32x4& dst) {
        asm volatile("global_load_dwordx4 %0, %1, off" : "=v"(dst) : "v"(asrc + (size_t)ks * 1024) : "memory");
    };
#pragma unroll
    for (int i = 0; i < RING - 1; ++i) { issue(i, i); aload(i, areg[i]); }
#pragma unroll
    for (int ks = 0; ks < KSW; ++ks) {
        constexpr int dummy = 0; (void)dummy;
        const int slot = ks % RING;
        if (ks + RING - 1 < KSW) { issue(ks + RING - 1, (ks + RING - 1) % RING); aload(ks + RING - 1, areg[(ks + RING - 1) % RING]); }
        const int rem = (KSW - 1 - ks) < (RING - 1) ? (KSW - 1 - ks) : (RING - 1);
        switch (rem) {
            case 0: asm volatile("s_waitcnt vmcnt(0)" : "+v"(areg[slot]) :: "memory"); break;
            case 1: asm volatile("s_waitcnt vmcnt(6)" : "+v"(areg[slot]) :: "memory"); break;
            case 2: asm volatile("s_waitcnt vmcnt(12)" : "+v"(areg[slot]) :: "memory"); break;
            case 3: asm volatile("s_waitcnt vmcnt(18)" : "+v"(areg[slot]) :: "memory"); break;
            case 4: asm volatile("s_waitcnt vmcnt(24)" : "+v"(areg[slot]) :: "memory"); break;
            case 5: asm volatile("s_waitcnt vmcnt(30)" : "+v"(areg[slot]) :: "memory"); break;
            case 6: asm volatile("s_waitcnt vmcnt(36)" : "+v"(areg[slot]) :: "memory"); break;
            default: asm volatile("s_waitcnt vmcnt(42)" : "+v"(areg[slot]) :: "memory"); break;
        }
        const bf16x8 a = __builtin_bit_cast(bf16x8, areg[slot]);
        const lds_cptr sb = lds + slot * 5120 + boff;
#pragma unroll
        for (int u = 0; u < 5; ++u) {
            const bf16x8 b = cat8(ldstr(sb + u * 64), ldstr(sb + u * 64 + 8 * 320));
            acc[u] = MFMA32(b, a, acc[u]);
        }
        asm volatile("s_waitcnt lgkmcnt(0)" ::: "memory");
    }
    STAMP(af - af_off, KSW == 10 ? 2 : 1, 1);
    f32x4* red = (f32x4*)smem;
#pragma unroll
    for (int rnd = 0; rnd < 2; ++rnd) {
        BAR_L();
#pragma unroll
        for (int G = 0; G < 10; ++G) {
            const int GG = rnd * 10 + G, u = GG >> 2, gq = GG & 3;
            red[(wid * 10 + G) * 64 + lane] = (f32x4){acc[u][4 * gq], acc[u][4 * gq + 1], acc[u][4 * gq + 2], acc[u][4 * gq + 3]};
        }
        BAR_L();
        if ((wid >> 1) == rnd) {
#pragma unroll
            for (int i = 0; i < 5; ++i) {
                const int G = (wid & 1) * 5 + i;
                res[i] = (red[(0 * 10 + G) * 64 + lane] + red[(1 * 10 + G) * 64 + lane]) + (red[(2 * 10 + G) * 64 + lane] + red[(3 * 10 + G) * 64 + lane]);
            }
        }
    }
}

constexpr int AT_STG = 4 * KVT_B, AT_NSL = 4, K2_LDS = AT_STG * AT_NSL;
constexpr int N_ATT = 512;

DI void attn_block(int head, int qb, char* __restrict__ ws, char* smem) {
    const int tid = threadIdx.x, lane = tid & 63, wid = __builtin_amdgcn_readfirstlane(tid >> 6);
    const int hh = lane >> 5, r = lane & 31;
    const unsigned lds0 = (unsigned)(uintptr_t)smem;
    const lds_cptr lds = (lds_cptr)smem;
    const char* kvsrc = ws + WS_KV + (size_t)(head * 64 + wid) * KVT_B + lane * 16;
    auto issue = [&](int st) {
        const unsigned dst = lds0 + (st & 3) * AT_STG + wid * KVT_B;
#pragma unroll
        for (int i = 0; i < 3; ++i) glds16(kvsrc + (size_t)st * AT_STG + i * 1024, dst + i * 1024);
    };
    const int qt = qb * 4 + wid;
    STAMP(ws, 1, 0);
    const char* qsrc = ws + WS_Q + (size_t)(head * 64 + qt) * QT_B;
    const bf16x8 q1 = *(const bf16x8*)(qsrc + lane * 16);
    u32x2 q2u = *(const u32x2*)(qsrc + 1024 + r * 8);
    if (hh) q2u = (u32x2){0u, 0u};
    const int reff = r <= 20 ? r : (r < 28 ? 20 : 19);
    const int k1off = lane * 16, k2off = 1024 + lane * 8, voff = 1536 + (hh * 24 + reff) * 16;
    f32x16 zero16;
#pragma unroll
    for (int e = 0; e < 16; ++e) zero16[e] = 0.f;
    f32x16 o;
    int* flag = (int*)(smem + K2_LDS);

    auto set_m = [&](float m) {
        const unsigned a = cvtpk(-m, 0.f) & 0xffffu;
        const float r1 = -m - bf_lo(a);
        const unsigned b = cvtpk(r1, 0.f) & 0xffffu;
        const float r2 = r1 - bf_lo(b);
        const unsigned c = cvtpk(r2, 0.f) & 0xffffu;
        if (hh) q2u = (u32x2){a | (b << 16), c};
    };

    for (int pass = 0;; ++pass) {
        if (pass == 1) {
            if (hh) q2u = (u32x2){0u, 0u};
            float m = -INFINITY;
            for (int st = 0; st < 16; ++st) {
                BAR_L();
                issue(st);
                WAITV_BAR(0);
                const lds_cptr sb = lds + (st & 3) * AT_STG;
                for (int t = 0; t < 4; ++t) {
                    f32x16 s = MFMA32(lds128(sb + t * KVT_B + k1off), q1, zero16);
                    s = MFMA32K8(lds64(sb + t * KVT_B + k2off), __builtin_bit_cast(bf16x4, q2u), s);
#pragma unroll
                    for (int e = 0; e < 16; ++e) m = __builtin_fmaxf(m, s[e]);
                }
            }
            m = __builtin_fmaxf(m, xhalf(m));
            set_m(m);
            BAR_L();
        }
        issue(0); issue(1); issue(2);
        WAITV_BAR(3);
        if (pass == 0) {
            f32x16 s = MFMA32(lds128(lds + k1off), q1, zero16);
            s = MFMA32K8(lds64(lds + k2off), __builtin_bit_cast(bf16x4, q2u), s);
            float m = s[0];
#pragma unroll
            for (int e = 1; e < 16; ++e) m = __builtin_fmaxf(m, s[e]);
            m = __builtin_fmaxf(m, xhalf(m));
            set_m(m);
        }
        const bf16x4 q2 = __builtin_bit_cast(bf16x4, q2u);
        STAMP(ws, 1, 1);
        o = zero16;
        f32x16 s = MFMA32(lds128(lds + k1off), q1, zero16);
        s = MFMA32K8(lds64(lds + k2off), q2, s);
        for (int st = 0; st < 16; ++st) {
            if (st > 0) {
                if (st < 14) { WAITV_BAR(3); } else { WAITV_BAR(0); }
            }
            if (st < 13) issue(st + 3);
            const lds_cptr sb = lds + (st & 3) * AT_STG;
            const lds_cptr sn = lds + ((st + 1) & 3) * AT_STG;
#pragma unroll
            for (int t = 0; t < 4; ++t) {
                const lds_cptr kn = t < 3 ? sb + (t + 1) * KVT_B : sn;
                const bf16x8 k1 = lds128(kn + k1off);
                const bf16x4 k2 = lds64(kn + k2off);
                const bf16x8 v0 = lds128(sb + t * KVT_B + voff), v1 = lds128(sb + t * KVT_B + voff + 768);
                f32x16 s2 = MFMA32(k1, q1, zero16);
                s2 = MFMA32K8(k2, q2, s2);
                u32x4 p0, p1;
#pragma unroll
                for (int e = 0; e < 4; ++e) p0[e] = cvtpk(__builtin_amdgcn_exp2f(s[2 * e]), __builtin_amdgcn_exp2f(s[2 * e + 1]));
                o = MFMA32(v0, __builtin_bit_cast(bf16x8, p0), o);
#pragma unroll
                for (int e = 0; e < 4; ++e) p1[e] = cvtpk(__builtin_amdgcn_exp2f(s[8 + 2 * e]), __builtin_amdgcn_exp2f(s[9 + 2 * e]));
                o = MFMA32(v1, __builtin_bit_cast(bf16x8, p1), o);
                s = s2;
            }
        }
        STAMP(ws, 1, 2);
        float l = o[8];
        { const float lx = xhalf(l); l = hh ? l : lx; }
        if (pass == 1) { o[8] = hh ? o[8] : o[8]; break; }
        bool bad = !(l < 1.0e18f) || !(l > 0.25f);
#pragma unroll
        for (int e = 0; e < 12; ++e) bad |= !(__builtin_fabsf(o[e]) < 3.0e38f) && (e < 8 || !hh);
        BAR_L();
        if (tid == 0) *flag = 0;
        BAR_L();
        if (bad) *flag = 1;
        BAR_L();
        if (*flag == 0) break;
    }
    float l = o[8];
    { const float lx = xhalf(l); l = hh ? l : lx; }
    const float il = 1.0f / l;
    char* obase = ws + WS_O + (size_t)qt * 40 * 1024 + r * 16;
#pragma unroll
    for (int i = 0; i < 3; ++i) {
        if (i < 2 || hh == 0) {
            const int G = 5 * head + 2 * i + hh;
            *(u32x2*)(obase + (G >> 2) * 1024 + (G & 1) * 512 + ((G >> 1) & 1) * 8) =
                (u32x2){cvtpk(o[4 * i] * il, o[4 * i + 1] * il), cvtpk(o[4 * i + 2] * il, o[4 * i + 3] * il)};
        }
    }
    STAMP(ws, 1, 3);
}

__global__ __launch_bounds__(256, 3) void k2_attn(const u16* __restrict__ w2, const u16* __restrict__ b2, char* __restrict__ ws) {
    extern __shared__ __attribute__((aligned(16))) char smem[];
    const int bid = blockIdx.x;
    if (bid < N_ATT) {
        const int g8 = bid & 7, idx = bid >> 3;
        attn_block(g8 * 4 + (idx >> 4), idx & 15, ws, smem);
    } else {
        const int b = bid - N_ATT, g8 = b & 7, idx = b >> 3;
        const int tt = g8 * 8 + (idx >> 2), nt = idx & 3;
        const int lane = threadIdx.x & 63, wid = __builtin_amdgcn_readfirstlane(threadIdx.x >> 6), hh = lane >> 5;
        f32x4 res[5];
        STAMP(ws, 1, 0);
        gemm32x160<27, 2>(ws + WS_H + (size_t)tt * 108 * 1024, WS_H + (size_t)tt * 108 * 1024, w2, nt * 160, smem, wid, lane, res);
        STAMP(ws, 1, 2);
        char* gdst = ws + WS_G + ((size_t)(tt * 4 + nt) * 20 + wid * 5) * 512 + lane * 8;
#pragma unroll
        for (int i = 0; i < 5; ++i) {
            const int G = wid * 5 + i, n = nt * 160 + (G >> 2) * 32 + (G & 3) * 8 + 4 * hh;
            const u32x2 bb = *(const u32x2*)(b2 + n);
            float v[4] = {res[i][0] + bf_lo(bb[0]), res[i][1] + bf_hi(bb[0]), res[i][2] + bf_lo(bb[1]), res[i][3] + bf_hi(bb[1])};
#pragma unroll
            for (int e = 0; e < 4; ++e) v[e] = v[e] * __builtin_amdgcn_rcpf(1.0f + __builtin_amdgcn_exp2f(-LOG2E * v[e]));
            *(u32x2*)(gdst + i * 512) = (u32x2){cvtpk(v[0], v[1]), cvtpk(v[2], v[3])};
        }
    }
}

constexpr int K3_RING = 7, K3_LDS = 4 * K3_RING * 5120;
__global__ __launch_bounds__(256) void k3_out(const u16* __restrict__ x, const u16* __restrict__ wo, const char* __restrict__ ws, u16* __restrict__ out) {
    extern __shared__ __attribute__((aligned(16))) char smem[];
    const int b = blockIdx.x, g8 = b & 7, idx = b >> 3;
    const int tt = g8 * 8 + (idx >> 2), nt = idx & 3;
    const int lane = threadIdx.x & 63, wid = __builtin_amdgcn_readfirstlane(threadIdx.x >> 6), hh = lane >> 5, r = lane & 31;
    f32x4 res[5];
    STAMP(ws, 2, 0);
    gemm32x160<10, K3_RING>(ws + WS_O + (size_t)tt * 40 * 1024, WS_O + (size_t)tt * 40 * 1024, wo, nt * 160, smem, wid, lane, res);
    STAMP(ws, 2, 2);
    const char* gsrc = ws + WS_G + ((size_t)(tt * 4 + nt) * 20 + wid * 5) * 512 + lane * 8;
#pragma unroll
    for (int i = 0; i < 5; ++i) {
        const int G = wid * 5 + i, n = nt * 160 + (G >> 2) * 32 + (G & 3) * 8 + 4 * hh;
        const size_t eo = (size_t)(tt * 32 + r) * DM + n;
        const u32x2 gg = *(const u32x2*)(gsrc + i * 512);
        const u32x2 xx = *(const u32x2*)(x + eo);
        const float v0 = __builtin_fmaf(res[i][0], bf_lo(gg[0]), bf_lo(xx[0])), v1 = __builtin_fmaf(res[i][1], bf_hi(gg[0]), bf_hi(xx[0]));
        const float v2 = __builtin_fmaf(res[i][2], bf_lo(gg[1]), bf_lo(xx[1])), v3 = __builtin_fmaf(res[i][3], bf_hi(gg[1]), bf_hi(xx[1]));
        *(u32x2*)(out + eo) = (u32x2){cvtpk(v0, v1), cvtpk(v2, v3)};
    }
    STAMP(ws, 2, 3);
}

extern "C" void kernel_launch(void* const* d_in, const int* in_sizes, int n_in, void* d_out, int out_size, void* d_ws, size_t ws_size,
                              hipStream_t stream) {
    const u16* x = (const u16*)d_in[0];
    const u16* wqkv = (const u16*)d_in[1];
    const u16* wo = (const u16*)d_in[2];
    const u16* w1 = (const u16*)d_in[4];
    const u16* b1 = (const u16*)d_in[5];
    const u16* w2 = (const u16*)d_in[6];
    const u16* b2 = (const u16*)d_in[7];
    char* ws = (char*)d_ws;
    (void)hipFuncSetAttribute((const void*)k1_proj, hipFuncAttributeMaxDynamicSharedMemorySize, K1_LDS);
    (void)hipFuncSetAttribute((const void*)k2_attn, hipFuncAttributeMaxDynamicSharedMemorySize, K2_LDS + 16);
    (void)hipFuncSetAttribute((const void*)k3_out, hipFuncAttributeMaxDynamicSharedMemorySize, K3_LDS);
    k1_proj<<<256, 512, K1_LDS, stream>>>(x, wqkv, w1, b1, ws);
    k2_attn<<<N_ATT + 256, 256, K2_LDS + 16, stream>>>(w2, b2, ws);
    k3_out<<<256, 256, K3_LDS, stream>>>(x, wo, ws, (u16*)d_out);
}
```

```cpp
#include <hip/hip_runtime.h>
#include <hip/hip_bf16.h>
#include <stdint.h>
#include <type_traits>

typedef short bf16x8 __attribute__((ext_vector_type(8)));
typedef short bf16x4 __attribute__((ext_vector_type(4)));
typedef float f32x16 __attribute__((ext_vector_type(16)));
typedef float f32x4 __attribute__((ext_vector_type(4)));
typedef unsigned u32x4 __attribute__((ext_vector_type(4)));
typedef unsigned u32x2 __attribute__((ext_vector_type(2)));
typedef __bf16 bf2_t __attribute__((ext_vector_type(2)));
typedef short v4i16_t __attribute__((ext_vector_type(4)));
typedef unsigned short u16;
#define LDS_AS __attribute__((address_space(3)))
typedef LDS_AS const char* lds_cptr;
typedef LDS_AS char* lds_ptr;
#define DI __device__ __forceinline__

constexpr int SEQ = 2048, DM = 640, NH = 32, DH = 20, FF = 1728;
constexpr float LN_EPS = 1e-5f;
constexpr float LOG2E = 1.4426950408889634f;
constexpr float QSCALE = 0.22360679774997896f * LOG2E;

constexpr int QT_B = 1280, KVT_B = 3072;
constexpr size_t WS_Q = 0;
constexpr size_t WS_KV = WS_Q + (size_t)NH * 64 * QT_B;
constexpr size_t WS_H = WS_KV + (size_t)NH * 64 * KVT_B;
constexpr size_t WS_O = WS_H + (size_t)128 * 54 * 1024;
constexpr size_t WS_W2F = WS_O + (size_t)128 * 20 * 1024;
constexpr size_t WS_WOF = WS_W2F + (size_t)40 * 54 * 1024;
constexpr size_t WS_FLAG = WS_WOF + (size_t)40 * 20 * 1024;
constexpr size_t WS_END = WS_FLAG + 8 * 64 * 4;
constexpr int AUX_WT = 16;

#define MFMA32(a, b, c) __builtin_amdgcn_mfma_f32_32x32x16_bf16((a), (b), (c), 0, 0, 0)
#define MFMA32K8(a, b, c) __builtin_amdgcn_mfma_f32_32x32x8bf16_1k((a), (b), (c), 0, 0, 0)

typedef float f32x2 __attribute__((ext_vector_type(2)));
DI unsigned cvtpk(float lo, float hi) { return __builtin_bit_cast(unsigned, __builtin_convertvector((f32x2){lo, hi}, bf2_t)); }
DI float bf_lo(unsigned u) { return __uint_as_float(u << 16); }
DI float bf_hi(unsigned u) { return __uint_as_float(u & 0xffff0000u); }
DI float dot2(unsigned a, unsigned b, float c) { return __builtin_amdgcn_fdot2_f32_bf16(__builtin_bit_cast(bf2_t, a), __builtin_bit_cast(bf2_t, b), c, false); }
DI void glds16(const void* g, unsigned lds_base) {
    unsigned sv;
    asm volatile("s_mov_b32 %0, m0\n\ts_mov_b32 m0, %2\n\ts_nop 0\n\tglobal_load_lds_dwordx4 %1, off\n\ts_mov_b32 m0, %0"
                 : "=&s"(sv) : "v"(g), "s"(lds_base) : "memory");
}
DI bf16x8 lds128(lds_cptr p) { return *(const LDS_AS bf16x8*)p; }
DI bf16x4 lds64(lds_cptr p) { return *(const LDS_AS bf16x4*)p; }
DI bf16x4 ldstr(lds_cptr p) { return __builtin_bit_cast(bf16x4, __builtin_amdgcn_ds_read_tr16_b64_v4i16((LDS_AS v4i16_t*)p)); }
DI bf16x8 cat8(bf16x4 a, bf16x4 b) { return __builtin_shufflevector(a, b, 0, 1, 2, 3, 4, 5, 6, 7); }
DI float xhalf(float v) {
    auto r = __builtin_amdgcn_permlane32_swap(__float_as_uint(v), __float_as_uint(v), false, false);
    return (threadIdx.x & 32) ? __uint_as_float(r[0]) : __uint_as_float(r[1]);
}
#define STAMP(ws, kid, i)
#define WAITV(n) asm volatile("s_waitcnt vmcnt(" #n ")" ::: "memory")
#define WAITV_BAR(n) asm volatile("s_waitcnt vmcnt(" #n ") lgkmcnt(0)\n\ts_barrier" ::: "memory")
#define BAR_L() asm volatile("s_waitcnt lgkmcnt(0)\n\ts_barrier" ::: "memory")

constexpr int K1_AW = 3 * 8192;
constexpr int K1_BOFF = 4 * K1_AW, K1_LDS = 6 * K1_AW + 4096;

template <int N, class F> DI void static_for(F&& f) {
    if constexpr (N > 0) { static_for<N - 1>(f); f(std::integral_constant<int, N - 1>{}); }
}
DI void glds16s(const void* sbase, unsigned voff, unsigned lds_dst) {
    asm volatile("s_mov_b32 m0, %2\n\ts_nop 0\n\tglobal_load_lds_dwordx4 %0, %1" :: "v"(voff), "s"(sbase), "s"(lds_dst) : "memory");
}
DI void glds4s(const void* sbase, unsigned voff, unsigned lds_dst) {
    asm volatile("s_mov_b32 m0, %2\n\ts_nop 0\n\tglobal_load_lds_dword %0, %1" :: "v"(voff), "s"(sbase), "s"(lds_dst) : "memory");
}
DI int lane_id() {
    int l;
    asm volatile("v_mbcnt_lo_u32_b32 %0, -1, 0\n\tv_mbcnt_hi_u32_b32 %0, -1, %0" : "=v"(l));
    return l;
}
DI float vadd(float a, float b) { float d; asm("v_add_f32 %0, %1, %2" : "=v"(d) : "v"(a), "v"(b)); return d; }
DI float vmul(float a, float b) { float d; asm("v_mul_f32 %0, %1, %2" : "=v"(d) : "v"(a), "v"(b)); return d; }
DI float vfma(float a, float b, float c) { float d; asm("v_fma_f32 %0, %1, %2, %3" : "=v"(d) : "v"(a), "v"(b), "v"(c)); return d; }
#define SB0() __builtin_amdgcn_sched_barrier(0)

DI float gelu_tanh(float u) {
    const float c1 = -2.0f * 0.7978845608028654f * LOG2E, c2 = c1 * 0.044715f;
    const float t = u * __builtin_fmaf(u * u, c2, c1);
    return u * __builtin_amdgcn_rcpf(1.0f + __builtin_amdgcn_exp2f(t));
}

__global__ __launch_bounds__(512) void k1_proj(const u16* __restrict__ x, const u16* __restrict__ wqkv, const u16* __restrict__ w1,
                                               const u16* __restrict__ b1, const u16* __restrict__ w2, const u16* __restrict__ wo,
                                               char* __restrict__ ws) {
    extern __shared__ __attribute__((aligned(16))) char smem[];
    const int tid = threadIdx.x, lane = tid & 63, wid = __builtin_amdgcn_readfirstlane(tid >> 6);
    const int g8 = blockIdx.x & 7, idx = blockIdx.x >> 3;
    const int nq = g8 >> 1, pi = idx >> 2;
    const auto rsrc = __builtin_amdgcn_make_buffer_rsrc(ws, 0, (int)WS_END, 0x00020000);
    if (pi >= (nq == 3 ? 8 : 7)) {
        const int wv = (g8 * 4 + (idx & 3)) * 8 + wid;
        STAMP(ws, 0, 8);
        const unsigned ldsw = (unsigned)(uintptr_t)smem + wid * 10240;
        const int g = lane >> 4, q = (lane & 15) >> 2, p = lane & 3;
        for (int un = wv; un < 296; un += 192) {
            const int ksg = un >> 2, qr = un & 3;
            const bool is2 = ksg < 54;
            const int ks = is2 ? ksg : ksg - 54, nks = is2 ? 54 : 20;
            const u16* src = (is2 ? w2 : wo) + (size_t)ks * 32 * DM + qr * 160;
#pragma unroll
            for (int i = 0; i < 10; ++i) {
                const int o = i * 64 + lane, row = o / 20, ch = o - row * 20;
                glds16s(src, row * (DM * 2) + ch * 16, ldsw + i * 1024);
            }
            const lds_cptr tp = (lds_cptr)smem + wid * 10240 + ((is2 ? 16 * (g & 1) + 4 * (g >> 1) : 8 * g) + q) * 320 + p * 8;
            const int second = is2 ? 8 * 320 : 4 * 320;
            WAITV(0);
            const int dst = (int)(is2 ? WS_W2F : WS_WOF) + (qr * 10 * nks + ks) * 1024 + lane * 16;
#pragma unroll
            for (int u = 0; u < 10; ++u)
                __builtin_amdgcn_raw_buffer_store_b128(__builtin_bit_cast(u32x4, cat8(ldstr(tp + u * 32), ldstr(tp + u * 32 + second))),
                                                       rsrc, dst + u * nks * 1024, 0, AUX_WT);
            asm volatile("s_waitcnt lgkmcnt(0)" ::: "memory");
        }
        STAMP(ws, 0, 9);
        if (wv < 2) __builtin_amdgcn_raw_buffer_store_b128((u32x4){0u, 0u, 0u, 0u}, rsrc, (int)WS_FLAG + wv * 1024 + lane * 16, 0, AUX_WT);
        for (int i = wv * 64 + lane; i < 2048 * 24; i += 192 * 64) {
            const int tile = i / 24, c = i - tile * 24;
            const int off = c < 16 ? 1280 + c * 16 : 1536 + (((c - 16) >> 1) * 24 + 20 + (c & 1)) * 16;
            const unsigned v0 = c < 16 || !(c & 1) ? 0x3f803f80u : 0u, v1 = c < 16 ? 0x00003f80u : v0;
            __builtin_amdgcn_raw_buffer_store_b128((u32x4){v0, v1, v0, v1}, rsrc, (int)WS_KV + tile * KVT_B + off, 0, AUX_WT);
        }
        STAMP(ws, 0, 10);
        STAMP(ws, 0, 11);
        return;
    }
    const int mt = (g8 & 1) * 4 + (idx & 3), cb = nq * 7 + pi;
    const int m0 = mt * 256;
    STAMP(ws, 0, 0);
    const bool mlp0 = cb < 27;
    const int t0 = mlp0 ? cb : (cb == 27 ? 27 : 29), t1 = mlp0 ? cb : (cb == 27 ? 28 : 29);
    const u16* B0 = (mlp0 ? w1 : wqkv) + t0 * 64;
    const u16* B1 = wqkv + t1 * 64;
    const int ld0 = mlp0 ? FF : 3 * DM;
    const u16* A0 = x + (size_t)m0 * DM;
    unsigned avo[4], bvo[2];
#pragma unroll
    for (int i = 0; i < 4; ++i) {
        const int row = wid * 32 + i * 8 + (lane >> 3);
        avo[i] = row * (DM * 2) + (((lane & 7) ^ ((row >> 1) & 7)) << 4);
    }
    {
        const int row = wid * 8 + (lane >> 3), ch = (lane & 7) ^ (((row >> 1) & 1) << 2);
        bvo[0] = row * ld0 * 2 + ch * 16;
        bvo[1] = row * (3 * DM * 2) + ch * 16;
    }
    const unsigned lds0 = (unsigned)(uintptr_t)smem;
    const lds_cptr lds = (lds_cptr)smem;
    const unsigned adst = lds0 + (wid >> 1) * K1_AW + (wid & 1) * 4096, bdst = lds0 + K1_BOFF + wid * 1024;
    auto dma = [&](int st, int j) {
        const int slot = st % 3;
        if (j < 4) glds16s(A0 + st * 64, avo[j], adst + slot * 8192 + j * 1024);
        else if (j == 4) glds16s(B0 + (size_t)st * 64 * ld0, bvo[0], bdst + slot * 8192);
        else glds16s(B1 + (size_t)st * 64 * (3 * DM), bvo[1], bdst + K1_AW + slot * 8192);
    };
#pragma unroll
    for (int st = 0; st < 3; ++st)
#pragma unroll
        for (int j = 0; j < 6; ++j) dma(st, j);

    const int wm = wid & 3, wn = wid >> 2, hh = lane >> 5, r = lane & 31;
    const int tq = wn ? t1 : t0;
    const bool is_mlp = mlp0 && wn == 0;
    const bool is_v = !is_mlp && tq >= 20;
    u32x2 bb[2][4];
#pragma unroll
    for (int ni = 0; ni < 2; ++ni)
#pragma unroll
        for (int gq = 0; gq < 4; ++gq) bb[ni][gq] = *(const u32x2*)(b1 + (mlp0 ? t0 * 64 : 0) + (ni ^ (wm & 1)) * 32 + 8 * gq + 4 * hh);
    f32x16 acc[2][2];
#pragma unroll
    for (int i = 0; i < 2; ++i)
#pragma unroll
        for (int j = 0; j < 2; ++j)
#pragma unroll
            for (int e = 0; e < 16; ++e) acc[i][j][e] = 0.f;
    float rs0 = 0.f, rs1 = 0.f, rq0 = 0.f, rq1 = 0.f, rs2 = 0.f, rq2 = 0.f, cs = 0.f;

    auto run = [&](auto TR) {
        constexpr bool TRANS = decltype(TR)::value;
        const int par = wm >> 1;
        lds_cptr ap[4][2];
#pragma unroll
        for (int kk = 0; kk < 4; ++kk)
#pragma unroll
            for (int mi = 0; mi < 2; ++mi)
                ap[kk][mi] = lds + wm * K1_AW + ((mi ^ wn) * 32 + r) * 128 + (((2 * (kk ^ par) + hh) ^ ((r >> 1) & 7)) << 4);
        const int g = lane >> 4, q = (lane & 15) >> 2, p = lane & 3;
        lds_cptr bp[2][2];
#pragma unroll
        for (int ni = 0; ni < 2; ++ni)
#pragma unroll
            for (int k1 = 0; k1 < 2; ++k1)
                bp[ni][k1] = lds + K1_BOFF + wn * K1_AW + (k1 ^ par) * 2048 + (8 * (g >> 1) + q) * 128 +
                             (((((ni ^ wm ^ (q >> 1)) & 1) << 2) + 2 * (g & 1) + (p >> 1)) << 4) + (p & 1) * 8;
        bf16x8 af[2][2], bfr[2][2];
        auto lda = [&](int st, int kk, int mi) { return lds128(ap[kk][mi] + (st % 3) * 8192); };
        auto ldb = [&](int st, int kk, int ni) {
            const lds_cptr s = bp[ni][kk & 1] + (st % 3) * 8192 + (kk & 2) * 2048;
            return cat8(ldstr(s), ldstr(s + 512));
        };
        auto stat = [&](unsigned u) {
            const float lo = bf_lo(u), hi = bf_hi(u);
            rs0 = vadd(rs0, lo); rq0 = vfma(lo, lo, rq0);
            rs1 = vadd(rs1, hi); rq1 = vfma(hi, hi, rq1);
        };
        WAITV_BAR(20);
        STAMP(ws, 0, 1);
        af[0][0] = lda(0, 0, 0); af[0][1] = lda(0, 0, 1); bfr[0][0] = ldb(0, 0, 0); bfr[0][1] = ldb(0, 0, 1);
#define K1_NV 3
        constexpr int NV = K1_NV;
        unsigned sa[4][4], sbv[2][4];
        auto clump = [&](int first) {
#pragma unroll
            for (int k = first; k < 4; ++k)
#pragma unroll
                for (int j = NV; j < 4; ++j) { rs2 = dot2(sa[k][j], 0x3f803f80u, rs2); rq2 = dot2(sa[k][j], sa[k][j], rq2); }
#pragma unroll
            for (int k = 0; k < 2; ++k)
#pragma unroll
                for (int j = 0; j < 4; ++j) cs = dot2(sbv[k][j], 0x3f803f80u, cs);
            SB0();
        };
        static_for<40>([&](auto S) {
            constexpr int s = decltype(S)::value, st = s >> 2, kk = s & 3, c = s & 1, n = c ^ 1;
            constexpr int ns = s + 1, nst = ns >> 2, nkk = ns & 3;
            if constexpr (kk == 3) clump(st == 0 ? 1 : 0);
            if constexpr (kk == 3 && st < 9) {
                if constexpr (st == 0) { WAITV_BAR(14); } else
                if constexpr (st < 8) { WAITV_BAR(6); } else { WAITV_BAR(0); }
            }
            constexpr int ds = (s + 1) / 4 + 2, dj = (s + 1) & 3;
            constexpr bool dm = s >= 3 && ds < 10;
            const u32x4 a = __builtin_bit_cast(u32x4, af[c][0]);
#pragma unroll
            for (int j = 0; j < 4; ++j) sa[(kk + 1) & 3][j] = a[j];
            if constexpr ((kk & 1) == 0) {
                const u32x4 b = __builtin_bit_cast(u32x4, bfr[c][0]);
#pragma unroll
                for (int j = 0; j < 4; ++j) sbv[kk >> 1][j] = b[j];
            }
#define K1_MM(mi, ni) acc[mi][ni] = TRANS ? MFMA32(bfr[c][ni], af[c][mi], acc[mi][ni]) : MFMA32(af[c][mi], bfr[c][ni], acc[mi][ni])
            K1_MM(0, 0);
            if constexpr (ns < 40) af[n][0] = lda(nst, nkk, 0);
            if constexpr (NV >= 1) stat(a[0]);
            SB0();
            K1_MM(0, 1);
            if constexpr (ns < 40) bfr[n][0] = ldb(nst, nkk, 0);
            if constexpr (dm) dma(ds, dj < 2 ? 2 * dj : dj + 2);
            if constexpr (NV >= 4) stat(a[3]);
            SB0();
            K1_MM(1, 0);
            if constexpr (ns < 40) bfr[n][1] = ldb(nst, nkk, 1);
            if constexpr (NV >= 2) stat(a[1]);
            SB0();
            K1_MM(1, 1);
            if constexpr (ns < 40) af[n][1] = lda(nst, nkk, 1);
            if constexpr (dm && dj < 2) dma(ds, 2 * dj + 1);
            if constexpr (NV >= 3) stat(a[2]);
            SB0();
        });
#pragma unroll
        for (int j = NV; j < 4; ++j) { rs2 = dot2(sa[0][j], 0x3f803f80u, rs2); rq2 = dot2(sa[0][j], sa[0][j], rq2); }
    };
    if (is_v) run(std::integral_constant<bool, false>{}); else run(std::integral_constant<bool, true>{});

    STAMP(ws, 0, 3);
    float* st_mu = (float*)(smem + 6 * K1_AW);
    float* st_r1 = st_mu + 256;
    float* st_r2 = st_r1 + 256;
    float* st_cs = st_r2 + 256;
    float rs = rs0 + rs1 + rs2, rq = rq0 + rq1 + rq2;
    rs += xhalf(rs); rq += xhalf(rq); cs += xhalf(cs);
    {
        const float mu = rs * (1.0f / DM);
        const float var = __builtin_fmaxf(rq * (1.0f / DM) - mu * mu, 0.f);
        const float r1 = __builtin_amdgcn_rsqf(var + LN_EPS);
        const float r12 = r1 * __builtin_amdgcn_rsqf(var * r1 * r1 + LN_EPS);
        float* pa = hh ? st_r2 + wm * 64 + wn * 32 + r : st_mu + wm * 64 + wn * 32 + r;
        float* pb = hh ? st_cs + (wm >> 1) * 128 + wn * 64 + (wm & 1) * 32 + r : st_r1 + wm * 64 + wn * 32 + r;
        *pa = hh ? r12 : mu;
        *pb = hh ? cs : r1;
    }
    BAR_L();
    STAMP(ws, 0, 4);
    if (cb == 28 && wn == 1) return;
    if (!is_mlp) __builtin_amdgcn_s_setprio(3);
    const int n0 = tq * 64;

    if (is_v) {
        f32x4 mu[2][4], sc[2][4];
#pragma unroll
        for (int mi = 0; mi < 2; ++mi)
#pragma unroll
            for (int q4 = 0; q4 < 4; ++q4) {
                mu[mi][q4] = *(const f32x4*)(st_mu + wm * 64 + (mi ^ wn) * 32 + 8 * q4 + 4 * hh);
                sc[mi][q4] = *(const f32x4*)(st_r1 + wm * 64 + (mi ^ wn) * 32 + 8 * q4 + 4 * hh);
            }
#pragma unroll
        for (int ni = 0; ni < 2; ++ni) {
            const int cl = (ni ^ (wm & 1)) * 32 + r;
            const float c = st_cs[wn * 64 + cl] + st_cs[128 + wn * 64 + cl];
            const int nn = n0 - 2 * DM + cl, head = nn / DH, d = nn - head * DH;
#pragma unroll
            for (int mi = 0; mi < 2; ++mi) {
                const int rl = wm * 64 + (mi ^ wn) * 32;
                const int dst = (int)WS_KV + (head * 64 + ((m0 + rl) >> 5)) * KVT_B + 1536 + (hh * 24 + d) * 16;
#pragma unroll
                for (int s = 0; s < 2; ++s) {
                    u32x4 o;
#pragma unroll
                    for (int gq = 0; gq < 2; ++gq) {
                        const f32x4 a = {acc[mi][ni][8 * s + 4 * gq], acc[mi][ni][8 * s + 4 * gq + 1], acc[mi][ni][8 * s + 4 * gq + 2], acc[mi][ni][8 * s + 4 * gq + 3]};
                        const f32x4 v = (a - mu[mi][2 * s + gq] * c) * sc[mi][2 * s + gq];
                        o[2 * gq] = cvtpk(v[0], v[1]); o[2 * gq + 1] = cvtpk(v[2], v[3]);
                    }
                    __builtin_amdgcn_raw_buffer_store_b128(o, rsrc, dst + s * 768, 0, AUX_WT);
                }
            }
        }
    } else {
        const bool isk = tq >= 10;
        float sc[2], cm[2];
#pragma unroll
        for (int mi = 0; mi < 2; ++mi) {
            const int rl = wm * 64 + (mi ^ wn) * 32 + r;
            sc[mi] = is_mlp ? st_r2[rl] : st_r1[rl] * (isk ? 1.0f : QSCALE);
            cm[mi] = -st_mu[rl] * sc[mi];
        }
        f32x4 cc[2][4];
#pragma unroll
        for (int ni = 0; ni < 2; ++ni)
#pragma unroll
            for (int gq = 0; gq < 4; ++gq) {
                const int cl = wn * 64 + (ni ^ (wm & 1)) * 32 + 8 * gq + 4 * hh;
                cc[ni][gq] = *(const f32x4*)(st_cs + cl) + *(const f32x4*)(st_cs + 128 + cl);
            }
        if (is_mlp) {
            float c1 = -2.0f * 0.7978845608028654f * LOG2E, c2 = c1 * 0.044715f;
            asm volatile("" : "+v"(c1), "+v"(c2));
#pragma unroll
            for (int ni = 0; ni < 2; ++ni) {
#pragma unroll
                for (int mi = 0; mi < 2; ++mi) {
                    unsigned pk[8];
#pragma unroll
                    for (int gq = 0; gq < 4; ++gq) {
                        const f32x4 bv = {bf_lo(bb[ni][gq][0]), bf_hi(bb[ni][gq][0]), bf_lo(bb[ni][gq][1]), bf_hi(bb[ni][gq][1])};
                        const f32x4 a = {acc[mi][ni][4 * gq], acc[mi][ni][4 * gq + 1], acc[mi][ni][4 * gq + 2], acc[mi][ni][4 * gq + 3]};
                        const f32x4 u = a * sc[mi] + (cc[ni][gq] * cm[mi] + bv);
                        const f32x4 t = u * (u * u * c2 + c1);
                        f32x4 d;
#pragma unroll
                        for (int e = 0; e < 4; ++e) d[e] = __builtin_amdgcn_exp2f(t[e]);
                        d = d + 1.0f;
#pragma unroll
                        for (int e = 0; e < 4; ++e) d[e] = __builtin_amdgcn_rcpf(d[e]);
                        const f32x4 v = u * d;
                        pk[2 * gq] = cvtpk(v[0], v[1]); pk[2 * gq + 1] = cvtpk(v[2], v[3]);
                    }
                    const int t16 = (m0 + wm * 64 + mi * 32 + r) >> 4;
                    const int dst = (int)WS_H + (t16 * 54 + ((n0 + (ni ^ (wm & 1)) * 32) >> 5)) * 1024 + (hh * 32 + (r & 15)) * 16;
                    __builtin_amdgcn_raw_buffer_store_b128((u32x4){pk[0], pk[1], pk[2], pk[3]}, rsrc, dst, 0, AUX_WT);
                    __builtin_amdgcn_raw_buffer_store_b128((u32x4){pk[4], pk[5], pk[6], pk[7]}, rsrc, dst + 256, 0, AUX_WT);
                }
            }
        } else {
            const int tb = isk ? KVT_B : QT_B;
            const int base = (int)(isk ? WS_KV : WS_Q) + ((m0 + wm * 64) >> 5) * tb;
#pragma unroll
            for (int ni = 0; ni < 2; ++ni)
#pragma unroll
                for (int gq = 0; gq < 4; ++gq) {
                    const int P = ((n0 - (isk ? DM : 0) + (ni ^ (wm & 1)) * 32) >> 2) + 2 * gq + hh;
                    const int head = P / 5, pc = P - head * 5;
                    const bool odd = r & 1;
                    const int off = base + head * 64 * tb + (pc < 4 ? (pc & 1) * 512 + (pc >> 1) * 256 : 1024) + (r & ~1) * 8 + ((r & 1) ^ wn) * tb;
                    unsigned w[2][2];
#pragma unroll
                    for (int mi = 0; mi < 2; ++mi) {
                        const f32x4 a = {acc[mi][ni][4 * gq], acc[mi][ni][4 * gq + 1], acc[mi][ni][4 * gq + 2], acc[mi][ni][4 * gq + 3]};
                        const f32x4 v = a * sc[mi] + cc[ni][gq] * cm[mi];
                        w[mi][0] = cvtpk(v[0], v[1]); w[mi][1] = cvtpk(v[2], v[3]);
                    }
                    const unsigned r0 = __builtin_amdgcn_mov_dpp(odd ? w[0][0] : w[1][0], 0xB1, 0xf, 0xf, true);
                    const unsigned r1 = __builtin_amdgcn_mov_dpp(odd ? w[0][1] : w[1][1], 0xB1, 0xf, 0xf, true);
                    __builtin_amdgcn_raw_buffer_store_b128((u32x4){odd ? r0 : w[0][0], odd ? r1 : w[0][1], odd ? w[1][0] : r0, odd ? w[1][1] : r1},
                                                           rsrc, off, 0, AUX_WT);
                }
        }
    }
    STAMP(ws, 0, 5);
    STAMP(ws, 0, 6);
}

constexpr int AT_STG = 8 * KVT_B, K2_LDS = 8 * 20 * 1024;
constexpr int GREG = 4 * 1280;
typedef __attribute__((address_space(1))) unsigned gu32;
#define BARRIER() asm volatile("s_barrier" ::: "memory")
DI void mfma16(f32x4& c, bf16x8 a, bf16x8 b, bool first) {
    if (first) asm volatile("v_mfma_f32_16x16x32_bf16 %0, %1, %2, 0" : "=&v"(c) : "v"(a), "v"(b));
    else asm volatile("v_mfma_f32_16x16x32_bf16 %0, %1, %2, %0" : "+v"(c) : "v"(a), "v"(b));
}

__global__ __launch_bounds__(768) void k2_attn(const u16* __restrict__ x, const u16* __restrict__ b2, char* __restrict__ ws,
                                               u16* __restrict__ out) {
    extern __shared__ __attribute__((aligned(16))) char smem[];
    const int tid = threadIdx.x, lane = tid & 63, wid = __builtin_amdgcn_readfirstlane(tid >> 6);
    const int hh = lane >> 5, r = lane & 31;
    const int g8 = blockIdx.x & 7, idx = blockIdx.x >> 3;
    const int head = g8 * 4 + (idx >> 3), qt = (idx & 7) * 8 + wid;
    const int tb = g8 * 4 + (idx >> 3), nb = idx & 7;
    const unsigned lds0 = (unsigned)(uintptr_t)smem;
    const lds_cptr lds = (lds_cptr)smem;
    const unsigned l16 = lane * 16;
    const auto rsrc = __builtin_amdgcn_make_buffer_rsrc(ws, 0, (int)WS_END, 0x00020000);
    gu32* flags = (gu32*)(ws + WS_FLAG);
    f32x4* red = (f32x4*)smem;
    auto rpos = [&](int reg, int i, int u) { return (reg * 20 + i * 5 + u) * 64 + (lane ^ (2 * u + hh)); };
    auto cpos = [&](int row, int ch) { return ((row >> 4) * 5 + (ch >> 1)) * 64 + (ch & 1) * 32 + ((row & 15) ^ ch); };
    STAMP(ws, 1, 0);
    if (wid >= 8) {
        const int gw = wid - 8, k0 = gw < 2 ? gw * 14 : gw * 13 + 2;
        const int abase = (int)WS_H + (tb * 4 * 54 + k0) * 1024, wbase = (int)WS_W2F + (nb * 5 * 54 + k0) * 1024;
        const auto rs_t = __builtin_amdgcn_make_buffer_rsrc(ws, 0, gw < 2 ? (int)WS_END : 0, 0x00020000);
        bf16x8 af[2][4], wf[2][5];
        f32x4 acc[4][5];
        auto ld = [&](int j) {
#pragma unroll
            for (int u = 0; u < 5; ++u) {
                wf[j & 1][u] = __builtin_bit_cast(bf16x8, __builtin_amdgcn_raw_buffer_load_b128(j == 13 ? rs_t : rsrc, l16, wbase + (u * 54 + j) * 1024, 0));
                if (u < 4) af[j & 1][u] = __builtin_bit_cast(bf16x8, __builtin_amdgcn_raw_buffer_load_b128(j == 13 ? rs_t : rsrc, l16, abase + (u * 54 + j) * 1024, 0));
            }
            SB0();
        };
        ld(0);
        u32x2 bb[5];
#pragma unroll
        for (int u = 0; u < 5; ++u) bb[u] = *(const u32x2*)(b2 + nb * 80 + u * 16 + (lane >> 4) * 4);
#pragma unroll
        for (int j = 0; j < 14; ++j) {
            if ((j & 1) == 0) BARRIER();
            if (j == 12) { BARRIER(); BARRIER(); }
            if (j == 0) {
                f32x4 bv[5];
#pragma unroll
                for (int u = 0; u < 5; ++u) {
                    if (gw) bb[u] = (u32x2){0u, 0u};
                    bv[u] = (f32x4){bf_lo(bb[u][0]), bf_hi(bb[u][0]), bf_lo(bb[u][1]), bf_hi(bb[u][1])};
                }
                asm volatile("s_nop 3" : "+v"(bv[0]), "+v"(bv[1]), "+v"(bv[2]), "+v"(bv[3]), "+v"(bv[4]));
#pragma unroll
                for (int i = 0; i < 4; ++i)
#pragma unroll
                    for (int u = 0; u < 5; ++u)
                        asm volatile("v_mfma_f32_16x16x32_bf16 %0, %1, %2, %3" : "=&v"(acc[i][u]) : "v"(wf[0][u]), "v"(af[0][i]), "v"(bv[u]));
            } else {
#pragma unroll
                for (int i = 0; i < 4; ++i)
#pragma unroll
                    for (int u = 0; u < 5; ++u) mfma16(acc[i][u], wf[j & 1][u], af[j & 1][i], false);
            }
            SB0();
            if (j == 0) ld(1);
            if (j + 2 < 14) ld(j + 2);
        }
        asm volatile("s_nop 15\n\ts_nop 15");
        SB0();
#pragma unroll
        for (int i = 0; i < 4; ++i)
#pragma unroll
            for (int u = 0; u < 5; ++u) red[GREG + rpos(gw, i, u)] = acc[i][u];
        STAMP(ws, 1, 2);
        BAR_L();
        f32x4* src = red + GREG + gw * 64 + lane;
        f32x4 pt[5][4];
#pragma unroll
        for (int c = 0; c < 5; ++c)
#pragma unroll
            for (int w = 0; w < 4; ++w) pt[c][w] = src[c * 256 + w * 1280];
        SB0();
#pragma unroll
        for (int c = 0; c < 5; ++c) {
            f32x4 g = (pt[c][0] + pt[c][1]) + (pt[c][2] + pt[c][3]);
#pragma unroll
            for (int e = 0; e < 4; ++e) g[e] = g[e] * __builtin_amdgcn_rcpf(1.0f + __builtin_amdgcn_exp2f(-LOG2E * g[e]));
            src[c * 256 + 1280] = g;
        }
        STAMP(ws, 1, 5);
        BAR_L();
        return;
    }
    {
    __builtin_amdgcn_s_setprio(2);
    const char* kvsrc = ws + WS_KV + (size_t)(head * 64 + wid) * KVT_B;
    auto dma = [&](int st, int i) { glds16s(kvsrc + (size_t)st * AT_STG + i * 1024, l16, lds0 + (st % 3) * AT_STG + wid * KVT_B + i * 1024); };
    const char* qsrc = ws + WS_Q + (size_t)(head * 64 + qt) * QT_B;
    u32x2 q1a, q1b, q2u;
    asm volatile("global_load_dwordx2 %0, %1, %2" : "=v"(q1a) : "v"(lane * 8), "s"(qsrc) : "memory");
    asm volatile("global_load_dwordx2 %0, %1, %2 offset:512" : "=v"(q1b) : "v"(lane * 8), "s"(qsrc) : "memory");
    asm volatile("global_load_dwordx2 %0, %1, %2 offset:1024" : "=v"(q2u) : "v"(r * 8), "s"(qsrc) : "memory");
#pragma unroll
    for (int st = 0; st < 3; ++st)
#pragma unroll
        for (int i = 0; i < 3; ++i) dma(st, i);

    const int mh = wid & 1, kq = wid >> 1;
    const int abase = (int)WS_O + ((tb * 4 + mh * 2) * 20 + kq * 5) * 1024, wbase = (int)WS_WOF + (nb * 5 * 20 + kq * 5) * 1024;
    bf16x8 af[5][2], wf[4][5];
    f32x4 acc[2][5];
    u32x4 xx[2];
    auto ldw1 = [&](int j, int u) {
        wf[j & 3][u] = __builtin_bit_cast(bf16x8, __builtin_amdgcn_raw_buffer_load_b128(rsrc, l16, wbase + (u * 20 + j) * 1024, 0));
    };
    auto ldw = [&](int j) {
#pragma unroll
        for (int u = 0; u < 5; ++u) ldw1(j, u);
    };
    const int reff = r < 21 ? r : 21;
    const lds_cptr k1p = lds + lane * 8, k2p = lds + 1024 + lane * 8, vp = lds + 1536 + (hh * 24 + reff) * 16;
    auto toff = [](int T) { return ((T >> 3) % 3) * AT_STG + (T & 7) * KVT_B; };
    auto ldk1 = [&](int T) { return cat8(lds64(k1p + toff(T)), lds64(k1p + toff(T) + 512)); };
    f32x16 zero16;
#pragma unroll
    for (int e = 0; e < 16; ++e) zero16[e] = 0.f;
    f32x16 o = zero16;

    auto set_m = [&](float m) {
        const unsigned a = cvtpk(-m, 0.f) & 0xffffu;
        const float r1 = -m - bf_lo(a);
        const unsigned b = cvtpk(r1, 0.f) & 0xffffu;
        const float r2 = r1 - bf_lo(b);
        const unsigned c = cvtpk(r2, 0.f) & 0xffffu;
        if (hh) q2u = (u32x2){a | (b << 16), c};
    };

    asm volatile("s_waitcnt vmcnt(6)\n\ts_barrier" : "+v"(q1a), "+v"(q1b), "+v"(q2u) :: "memory");
    if (hh) q2u = (u32x2){0u, 0u};
    const bf16x8 q1 = __builtin_bit_cast(bf16x8, (u32x4){q1a[0], q1a[1], q1b[0], q1b[1]});
    {
        f32x16 s = MFMA32(ldk1(0), q1, zero16);
        s = MFMA32K8(lds64(k2p), __builtin_bit_cast(bf16x4, q2u), s);
        float m = s[0];
#pragma unroll
        for (int e = 1; e < 16; ++e) m = __builtin_fmaxf(m, s[e]);
        m = __builtin_fmaxf(m, xhalf(m));
        set_m(m);
    }
    STAMP(ws, 1, 1);
    {
        const bf16x4 q2 = __builtin_bit_cast(bf16x4, q2u);
        f32x16 sb[2];
        sb[0] = MFMA32(ldk1(0), q1, zero16);
        sb[0] = MFMA32K8(lds64(k2p), q2, sb[0]);
        bf16x8 k1 = ldk1(1), v0 = lds128(vp), v1;
        bf16x4 k2 = lds64(k2p + toff(1));
        u32x4 p0, p1;
        SB0();
        static_for<256>([&](auto GI) {
            constexpr int g = decltype(GI)::value, T = g >> 2, sl = g & 3, st = T >> 3;
            constexpr int c = T & 1, n = c ^ 1;
            if constexpr ((T & 7) == 6 && sl == 0 && st < 7) {
                if constexpr (st == 0) asm volatile("s_waitcnt vmcnt(3)\n\ts_barrier" ::: "memory");
                else asm volatile("s_waitcnt vmcnt(0)\n\ts_barrier" ::: "memory");
            }
            if constexpr (sl == 0) {
                if constexpr (T < 63) sb[n] = MFMA32(k1, q1, zero16);
                if constexpr (T < 62) k1 = ldk1(T + 2);
            } else if constexpr (sl == 1) {
                if constexpr (T > 0) o = MFMA32(v1, __builtin_bit_cast(bf16x8, p1), o);
                v1 = lds128(vp + toff(T) + 768);
            } else if constexpr (sl == 2) {
                if constexpr (T < 63) sb[n] = MFMA32K8(k2, q2, sb[n]);
                if constexpr (T < 62) k2 = lds64(k2p + toff(T + 2));
            } else {
                o = MFMA32(v0, __builtin_bit_cast(bf16x8, p0), o);
                if constexpr (T < 63) v0 = lds128(vp + toff(T + 1));
            }
            if constexpr ((T & 7) == 6 && sl < 3 && st >= 1 && st + 2 <= 7) dma(st + 2, sl);
#define NPW 3
            if constexpr (g >= 218 && g < 218 + 5 * NPW) ldw1((g - 218) / 5, (g - 218) % 5);
            SB0();
            const float e0 = __builtin_amdgcn_exp2f(sb[c][4 * sl]), e1 = __builtin_amdgcn_exp2f(sb[c][4 * sl + 1]);
            const float e2 = __builtin_amdgcn_exp2f(sb[c][4 * sl + 2]), e3 = __builtin_amdgcn_exp2f(sb[c][4 * sl + 3]);
            if constexpr (sl < 2) { p0[2 * sl] = cvtpk(e0, e1); p0[2 * sl + 1] = cvtpk(e2, e3); }
            else { p1[2 * sl - 4] = cvtpk(e0, e1); p1[2 * sl - 3] = cvtpk(e2, e3); }
            SB0();
        });
        o = MFMA32(v1, __builtin_bit_cast(bf16x8, p1), o);
    }
    STAMP(ws, 1, 2);
    float l = o[8];
    { const float lx = xhalf(l); l = hh ? l : lx; }
    bool bad = !(l < 1.0e18f) || !(l > 0.25f);
#pragma unroll
    for (int e = 0; e < 12; ++e) bad |= !(__builtin_fabsf(o[e]) < 3.0e38f) && (e < 8 || !hh);
    if (__builtin_amdgcn_ballot_w64(bad) != 0) {
        const char* kvh = ws + WS_KV + (size_t)head * 64 * KVT_B;
        for (int pass = 0; pass < 2; ++pass) {
            if (pass == 0 && hh) q2u = (u32x2){0u, 0u};
            float m = -INFINITY;
            o = zero16;
            for (int T = 0; T < 64; ++T) {
                const char* kt = kvh + T * KVT_B;
                f32x16 s = MFMA32(cat8(*(const bf16x4*)(kt + lane * 8), *(const bf16x4*)(kt + 512 + lane * 8)), q1, zero16);
                s = MFMA32K8(*(const bf16x4*)(kt + 1024 + lane * 8), __builtin_bit_cast(bf16x4, q2u), s);
                if (pass == 0) {
#pragma unroll
                    for (int e = 0; e < 16; ++e) m = __builtin_fmaxf(m, s[e]);
                } else {
                    u32x4 p0, p1;
#pragma unroll
                    for (int e = 0; e < 4; ++e) {
                        p0[e] = cvtpk(__builtin_amdgcn_exp2f(s[2 * e]), __builtin_amdgcn_exp2f(s[2 * e + 1]));
                        p1[e] = cvtpk(__builtin_amdgcn_exp2f(s[8 + 2 * e]), __builtin_amdgcn_exp2f(s[9 + 2 * e]));
                    }
                    const char* vt = kt + 1536 + (hh * 24 + reff) * 16;
                    o = MFMA32(*(const bf16x8*)vt, __builtin_bit_cast(bf16x8, p0), o);
                    o = MFMA32(*(const bf16x8*)(vt + 768), __builtin_bit_cast(bf16x8, p1), o);
                }
            }
            if (pass == 0) { m = __builtin_fmaxf(m, xhalf(m)); set_m(m); }
        }
        l = o[8];
        { const float lx = xhalf(l); l = hh ? l : lx; }
    }
    const float il = 1.0f / l;
    const int obase = (int)WS_O + (qt * 2 + (r >> 4)) * 20 * 1024 + (r & 15) * 16;
#pragma unroll
    for (int i = 0; i < 3; ++i) {
        if (i < 2 || hh == 0) {
            const int G = 5 * head + 2 * i + hh;
            __builtin_amdgcn_raw_buffer_store_b64((u32x2){cvtpk(o[4 * i] * il, o[4 * i + 1] * il), cvtpk(o[4 * i + 2] * il, o[4 * i + 3] * il)},
                                                  rsrc, obase + (G >> 3) * 1024 + ((G >> 1) & 3) * 256 + (G & 1) * 8, 0, AUX_WT);
        }
    }
    WAITV_BAR(0);
    if (tid == 448) __hip_atomic_store(flags + (idx & 7) * 64 + head, 1u, __ATOMIC_RELAXED, __HIP_MEMORY_SCOPE_AGENT);
    STAMP(ws, 1, 3);
#pragma unroll
    for (int j = NPW; j < 4; ++j) ldw(j);
    SB0();
    BARRIER();
    for (unsigned spin = 0; spin < (1u << 22); ++spin) {
        const unsigned f = __hip_atomic_load(flags + g8 * 64 + kq * 8 + (lane & 7), __ATOMIC_RELAXED, __HIP_MEMORY_SCOPE_AGENT);
        if (__builtin_amdgcn_ballot_w64(f == 1u) == ~0ull) break;
        __builtin_amdgcn_s_sleep(2);
    }
    __builtin_amdgcn_fence(__ATOMIC_ACQUIRE, "wavefront");
    STAMP(ws, 1, 4);
#pragma unroll
    for (int j = 0; j < 5; ++j)
#pragma unroll
        for (int i = 0; i < 2; ++i)
            af[j][i] = __builtin_bit_cast(bf16x8, __builtin_amdgcn_raw_buffer_load_b128(rsrc, l16, abase + (i * 20 + j) * 1024, AUX_WT));
    SB0();
#pragma unroll
    for (int j = 0; j < 5; ++j) {
#pragma unroll
        for (int i = 0; i < 2; ++i)
#pragma unroll
            for (int u = 0; u < 5; ++u) mfma16(acc[i][u], wf[j & 3][u], af[j][i], j == 0);
        SB0();
        if (j == 0) { ldw(4); SB0(); }
        if (j == 1) {
#pragma unroll
            for (int ps = 0; ps < 2; ++ps) {
                const int c = (wid < 2 ? ps * 512 : 0) + wid * 64 + lane_id(), row = c / 10, ch = c - row * 10;
                xx[ps] = *(const u32x4*)(x + (size_t)(tb * 64 + row) * DM + nb * 80 + ch * 8);
            }
            SB0();
        }
    }
    asm volatile("s_nop 15\n\ts_nop 15");
    SB0();
    STAMP(ws, 1, 5);
#pragma unroll
    for (int i = 0; i < 2; ++i)
#pragma unroll
        for (int u = 0; u < 5; ++u) red[rpos(kq, mh * 2 + i, u)] = acc[i][u];
    BAR_L();
    STAMP(ws, 1, 6);
#pragma unroll
    for (int ps = 0; ps < 2; ++ps) {
        if (ps == 0 || wid < 2) {
            const int c = ps * 512 + tid, row = c / 10, ch = c - row * 10;
            const f32x4* src = red + cpos(row, ch);
            const f32x4 s0 = (src[0] + src[1280]) + (src[2560] + src[3840]), s1 = (src[16] + src[1296]) + (src[2576] + src[3856]);
            const f32x4 g0 = src[GREG + 1280], g1 = src[GREG + 1296];
            const u32x4 xv = xx[ps];
            u32x4 ov;
            ov[0] = cvtpk(__builtin_fmaf(s0[0], g0[0], bf_lo(xv[0])), __builtin_fmaf(s0[1], g0[1], bf_hi(xv[0])));
            ov[1] = cvtpk(__builtin_fmaf(s0[2], g0[2], bf_lo(xv[1])), __builtin_fmaf(s0[3], g0[3], bf_hi(xv[1])));
            ov[2] = cvtpk(__builtin_fmaf(s1[0], g1[0], bf_lo(xv[2])), __builtin_fmaf(s1[1], g1[1], bf_hi(xv[2])));
            ov[3] = cvtpk(__builtin_fmaf(s1[2], g1[2], bf_lo(xv[3])), __builtin_fmaf(s1[3], g1[3], bf_hi(xv[3])));
            *(u32x4*)(out + (size_t)(tb * 64 + row) * DM + nb * 80 + ch * 8) = ov;
        }
    }
    }
    STAMP(ws, 1, 7);
}

extern "C" void kernel_launch(void* const* d_in, const int* in_sizes, int n_in, void* d_out, int out_size, void* d_ws, size_t ws_size,
                              hipStream_t stream) {
    const u16* x = (const u16*)d_in[0];
    const u16* wqkv = (const u16*)d_in[1];
    const u16* wo = (const u16*)d_in[2];
    const u16* w1 = (const u16*)d_in[4];
    const u16* b1 = (const u16*)d_in[5];
    const u16* w2 = (const u16*)d_in[6];
    const u16* b2 = (const u16*)d_in[7];
    char* ws = (char*)d_ws;
    (void)hipFuncSetAttribute((const void*)k1_proj, hipFuncAttributeMaxDynamicSharedMemorySize, K1_LDS);
    (void)hipFuncSetAttribute((const void*)k2_attn, hipFuncAttributeMaxDynamicSharedMemorySize, K2_LDS);
    k1_proj<<<256, 512, K1_LDS, stream>>>(x, wqkv, w1, b1, w2, wo, ws);
    k2_attn<<<256, 768, K2_LDS, stream>>>(x, b2, ws, (u16*)d_out);
}
```

```cpp
#define XB2
#include <hip/hip_runtime.h>
#include <hip/hip_bf16.h>
#include <stdint.h>
#include <type_traits>

typedef short bf16x8 __attribute__((ext_vector_type(8)));
typedef short bf16x4 __attribute__((ext_vector_type(4)));
typedef float f32x16 __attribute__((ext_vector_type(16)));
typedef float f32x4 __attribute__((ext_vector_type(4)));
typedef unsigned u32x4 __attribute__((ext_vector_type(4)));
typedef unsigned u32x2 __attribute__((ext_vector_type(2)));
typedef __bf16 bf2_t __attribute__((ext_vector_type(2)));
typedef short v4i16_t __attribute__((ext_vector_type(4)));
typedef unsigned short u16;
#define LDS_AS __attribute__((address_space(3)))
typedef LDS_AS const char* lds_cptr;
typedef LDS_AS char* lds_ptr;
#define DI __device__ __forceinline__

constexpr int SEQ = 2048, DM = 640, NH = 32, DH = 20, FF = 1728;
constexpr float LN_EPS = 1e-5f;
constexpr float LOG2E = 1.4426950408889634f;
constexpr float QSCALE = 0.22360679774997896f * LOG2E;

constexpr int QT_B = 1280, KVT_B = 3072;
constexpr size_t WS_Q = 0;
constexpr size_t WS_KV = WS_Q + (size_t)NH * 64 * QT_B;
constexpr size_t WS_H = WS_KV + (size_t)NH * 64 * KVT_B;
constexpr size_t WS_O = WS_H + (size_t)128 * 54 * 1024;
constexpr size_t WS_W2F = WS_O + (size_t)128 * 20 * 1024;
constexpr size_t WS_WOF = WS_W2F + (size_t)40 * 54 * 1024;
constexpr size_t WS_FLAG = WS_WOF + (size_t)40 * 20 * 1024;
constexpr size_t WS_END = WS_FLAG + 8 * 64 * 4;
constexpr int AUX_WT = 16;

#define MFMA32(a, b, c) __builtin_amdgcn_mfma_f32_32x32x16_bf16((a), (b), (c), 0, 0, 0)
#define MFMA32K8(a, b, c) __builtin_amdgcn_mfma_f32_32x32x8bf16_1k((a), (b), (c), 0, 0, 0)

typedef float f32x2 __attribute__((ext_vector_type(2)));
DI unsigned cvtpk(float lo, float hi) { return __builtin_bit_cast(unsigned, __builtin_convertvector((f32x2){lo, hi}, bf2_t)); }
DI float bf_lo(unsigned u) { return __uint_as_float(u << 16); }
DI float bf_hi(unsigned u) { return __uint_as_float(u & 0xffff0000u); }
DI float dot2(unsigned a, unsigned b, float c) { return __builtin_amdgcn_fdot2_f32_bf16(__builtin_bit_cast(bf2_t, a), __builtin_bit_cast(bf2_t, b), c, false); }
DI bf16x8 lds128(lds_cptr p) { return *(const LDS_AS bf16x8*)p; }
DI bf16x4 lds64(lds_cptr p) { return *(const LDS_AS bf16x4*)p; }
DI bf16x4 ldstr(lds_cptr p) { return __builtin_bit_cast(bf16x4, __builtin_amdgcn_ds_read_tr16_b64_v4i16((LDS_AS v4i16_t*)p)); }
DI bf16x8 cat8(bf16x4 a, bf16x4 b) { return __builtin_shufflevector(a, b, 0, 1, 2, 3, 4, 5, 6, 7); }
DI float xhalf(float v) {
    auto r = __builtin_amdgcn_permlane32_swap(__float_as_uint(v), __float_as_uint(v), false, false);
    return (threadIdx.x & 32) ? __uint_as_float(r[0]) : __uint_as_float(r[1]);
}
#define WAITV(n) asm volatile("s_waitcnt vmcnt(" #n ")" ::: "memory")
#define WAITV_BAR(n) asm volatile("s_waitcnt vmcnt(" #n ") lgkmcnt(0)\n\ts_barrier" ::: "memory")
#define BAR_L() asm volatile("s_waitcnt lgkmcnt(0)\n\ts_barrier" ::: "memory")

constexpr int K1_AW = 3 * 8192;
constexpr int K1_BOFF = 4 * K1_AW, K1_LDS = 6 * K1_AW + 4096;

template <int N, class F> DI void static_for(F&& f) {
    if constexpr (N > 0) { static_for<N - 1>(f); f(std::integral_constant<int, N - 1>{}); }
}
DI void glds16s(const void* sbase, unsigned voff, unsigned lds_dst) {
    asm volatile("s_mov_b32 m0, %2\n\ts_nop 0\n\tglobal_load_lds_dwordx4 %0, %1" :: "v"(voff), "s"(sbase), "s"(lds_dst) : "memory");
}
DI int lane_id() {
    int l;
    asm volatile("v_mbcnt_lo_u32_b32 %0, -1, 0\n\tv_mbcnt_hi_u32_b32 %0, -1, %0" : "=v"(l));
    return l;
}
DI float vadd(float a, float b) { float d; asm("v_add_f32 %0, %1, %2" : "=v"(d) : "v"(a), "v"(b)); return d; }
DI float vfma(float a, float b, float c) { float d; asm("v_fma_f32 %0, %1, %2, %3" : "=v"(d) : "v"(a), "v"(b), "v"(c)); return d; }
#define SB0() __builtin_amdgcn_sched_barrier(0)

__global__ __launch_bounds__(512) void k1_proj(const u16* __restrict__ x, const u16* __restrict__ wqkv, const u16* __restrict__ w1,
                                               const u16* __restrict__ b1, const u16* __restrict__ w2, const u16* __restrict__ wo,
                                               char* __restrict__ ws) {
    extern __shared__ __attribute__((aligned(16))) char smem[];
    const int tid = threadIdx.x, lane = tid & 63, wid = __builtin_amdgcn_readfirstlane(tid >> 6);
    const int g8 = blockIdx.x & 7, idx = blockIdx.x >> 3;
    const int nq = g8 >> 1, pi = idx >> 2;
    const auto rsrc = __builtin_amdgcn_make_buffer_rsrc(ws, 0, (int)WS_END, 0x00020000);
    if (pi >= (nq == 3 ? 8 : 7)) {
        const int wv = (g8 * 4 + (idx & 3)) * 8 + wid;
        const unsigned ldsw = (unsigned)(uintptr_t)smem + wid * 10240;
        const int g = lane >> 4, q = (lane & 15) >> 2, p = lane & 3;
        for (int un = wv; un < 296; un += 192) {
            const int ksg = un >> 2, qr = un & 3;
            const bool is2 = ksg < 54;
            const int ks = is2 ? ksg : ksg - 54, nks = is2 ? 54 : 20;
            const u16* src = (is2 ? w2 : wo) + (size_t)ks * 32 * DM + qr * 160;
#pragma unroll
            for (int i = 0; i < 10; ++i) {
                const int o = i * 64 + lane, row = o / 20, ch = o - row * 20;
                glds16s(src, row * (DM * 2) + ch * 16, ldsw + i * 1024);
            }
            const lds_cptr tp = (lds_cptr)smem + wid * 10240 + ((is2 ? 16 * (g & 1) + 4 * (g >> 1) : 8 * g) + q) * 320 + p * 8;
            const int second = is2 ? 8 * 320 : 4 * 320;
            WAITV(0);
            const int dst = (int)(is2 ? WS_W2F : WS_WOF) + (qr * 10 * nks + ks) * 1024 + lane * 16;
#pragma unroll
            for (int u = 0; u < 10; ++u)
                __builtin_amdgcn_raw_buffer_store_b128(__builtin_bit_cast(u32x4, cat8(ldstr(tp + u * 32), ldstr(tp + u * 32 + second))),
                                                       rsrc, dst + u * nks * 1024, 0, AUX_WT);
            asm volatile("s_waitcnt lgkmcnt(0)" ::: "memory");
        }
        if (wv < 2) __builtin_amdgcn_raw_buffer_store_b128((u32x4){0u, 0u, 0u, 0u}, rsrc, (int)WS_FLAG + wv * 1024 + lane * 16, 0, AUX_WT);
        for (int i = wv * 64 + lane; i < 2048 * 24; i += 192 * 64) {
            const int tile = i / 24, c = i - tile * 24;
            const int off = c < 16 ? 1280 + c * 16 : 1536 + (((c - 16) >> 1) * 24 + 20 + (c & 1)) * 16;
            const unsigned v0 = c < 16 || !(c & 1) ? 0x3f803f80u : 0u, v1 = c < 16 ? 0x00003f80u : v0;
            __builtin_amdgcn_raw_buffer_store_b128((u32x4){v0, v1, v0, v1}, rsrc, (int)WS_KV + tile * KVT_B + off, 0, AUX_WT);
        }
        return;
    }
    const int mt = (g8 & 1) * 4 + (idx & 3), cb = nq * 7 + pi;
    const int m0 = mt * 256;
    const bool mlp0 = cb < 27;
    const int t0 = mlp0 ? cb : (cb == 27 ? 27 : 29), t1 = mlp0 ? cb : (cb == 27 ? 28 : 29);
    const u16* B0 = (mlp0 ? w1 : wqkv) + t0 * 64;
    const u16* B1 = wqkv + t1 * 64;
    const int ld0 = mlp0 ? FF : 3 * DM;
    const u16* A0 = x + (size_t)m0 * DM;
    unsigned avo[4], bvo[2];
#pragma unroll
    for (int i = 0; i < 4; ++i) {
        const int row = wid * 32 + i * 8 + (lane >> 3);
        avo[i] = row * (DM * 2) + (((lane & 7) ^ ((row >> 1) & 7)) << 4);
    }
    {
        const int row = wid * 8 + (lane >> 3), ch = (lane & 7) ^ (((row >> 1) & 1) << 2);
        bvo[0] = row * ld0 * 2 + ch * 16;
        bvo[1] = row * (3 * DM * 2) + ch * 16;
    }
    const unsigned lds0 = (unsigned)(uintptr_t)smem;
    const lds_cptr lds = (lds_cptr)smem;
    const unsigned adst = lds0 + (wid >> 1) * K1_AW + (wid & 1) * 4096, bdst = lds0 + K1_BOFF + wid * 1024;
    auto dma = [&](int st, int j) {
        const int slot = st % 3;
        if (j < 4) glds16s(A0 + st * 64, avo[j], adst + slot * 8192 + j * 1024);
        else if (j == 4) glds16s(B0 + (size_t)st * 64 * ld0, bvo[0], bdst + slot * 8192);
        else glds16s(B1 + (size_t)st * 64 * (3 * DM), bvo[1], bdst + K1_AW + slot * 8192);
    };
#pragma unroll
    for (int st = 0; st < 3; ++st) {
#pragma unroll
        for (int j = 0; j < 6; ++j) dma(st, j);
    }

    const int wm = wid & 3, wn = wid >> 2, hh = lane >> 5, r = lane & 31;
    const int tq = wn ? t1 : t0;
    const bool is_mlp = mlp0 && wn == 0;
    const bool is_v = !is_mlp && tq >= 20;
    u32x2 bb[2][4];
#pragma unroll
    for (int ni = 0; ni < 2; ++ni)
#pragma unroll
        for (int gq = 0; gq < 4; ++gq) bb[ni][gq] = *(const u32x2*)(b1 + (mlp0 ? t0 * 64 : 0) + (ni ^ (wm & 1)) * 32 + 8 * gq + 4 * hh);
    f32x16 acc[2][2];
#pragma unroll
    for (int i = 0; i < 2; ++i)
#pragma unroll
        for (int j = 0; j < 2; ++j)
#pragma unroll
            for (int e = 0; e < 16; ++e) acc[i][j][e] = 0.f;
    float rs0 = 0.f, rs1 = 0.f, rq0 = 0.f, rq1 = 0.f, rs2 = 0.f, rq2 = 0.f, cs = 0.f;

    auto run = [&](auto TR) {
        constexpr bool TRANS = decltype(TR)::value;
        const int par = wm >> 1;
        lds_cptr ap[4][2];
#pragma unroll
        for (int kk = 0; kk < 4; ++kk)
#pragma unroll
            for (int mi = 0; mi < 2; ++mi)
                ap[kk][mi] = lds + wm * K1_AW + ((mi ^ wn) * 32 + r) * 128 + (((2 * (kk ^ par) + hh) ^ ((r >> 1) & 7)) << 4);
        const int g = lane >> 4, q = (lane & 15) >> 2, p = lane & 3;
        lds_cptr bp[2][2];
#pragma unroll
        for (int ni = 0; ni < 2; ++ni)
#pragma unroll
            for (int k1 = 0; k1 < 2; ++k1)
                bp[ni][k1] = lds + K1_BOFF + wn * K1_AW + (k1 ^ par) * 2048 + (8 * (g >> 1) + q) * 128 +
                             (((((ni ^ wm ^ (q >> 1)) & 1) << 2) + 2 * (g & 1) + (p >> 1)) << 4) + (p & 1) * 8;
        bf16x8 af[2][2], bfr[2][2];
        auto lda = [&](int st, int kk, int mi) { return lds128(ap[kk][mi] + (st % 3) * 8192); };
        auto ldb = [&](int st, int kk, int ni) {
            const lds_cptr s = bp[ni][kk & 1] + (st % 3) * 8192 + (kk & 2) * 2048;
            return cat8(ldstr(s), ldstr(s + 512));
        };
        auto stat = [&](unsigned u) {
            const float lo = bf_lo(u), hi = bf_hi(u);
            rs0 = vadd(rs0, lo); rq0 = vfma(lo, lo, rq0);
            rs1 = vadd(rs1, hi); rq1 = vfma(hi, hi, rq1);
        };
        WAITV_BAR(20);
        af[0][0] = lda(0, 0, 0); af[0][1] = lda(0, 0, 1); bfr[0][0] = ldb(0, 0, 0); bfr[0][1] = ldb(0, 0, 1);
        constexpr int NV = 3;
        unsigned sa[4][4], sbv[2][4];
        auto clump = [&](int first) {
#pragma unroll
            for (int k = first; k < 4; ++k)
#pragma unroll
                for (int j = NV; j < 4; ++j) { rs2 = dot2(sa[k][j], 0x3f803f80u, rs2); rq2 = dot2(sa[k][j], sa[k][j], rq2); }
#pragma unroll
            for (int k = 0; k < 2; ++k)
#pragma unroll
                for (int j = 0; j < 4; ++j) cs = dot2(sbv[k][j], 0x3f803f80u, cs);
            SB0();
        };
        static_for<40>([&](auto S) {
            constexpr int s = decltype(S)::value, st = s >> 2, kk = s & 3, c = s & 1, n = c ^ 1;
            constexpr int ns = s + 1, nst = ns >> 2, nkk = ns & 3;
            if constexpr (kk == 3) clump(st == 0 ? 1 : 0);
            if constexpr (kk == 3 && st < 9) {
                if constexpr (st == 0) { WAITV_BAR(14); } else
                if constexpr (st < 8) { WAITV_BAR(6); } else { WAITV_BAR(0); }
            }
            constexpr int ds = (s + 1) / 4 + 2, dj = (s + 1) & 3;
            constexpr bool dm = s >= 3 && ds < 10;
            const u32x4 a = __builtin_bit_cast(u32x4, af[c][0]);
#pragma unroll
            for (int j = 0; j < 4; ++j) sa[(kk + 1) & 3][j] = a[j];
            if constexpr ((kk & 1) == 0) {
                const u32x4 b = __builtin_bit_cast(u32x4, bfr[c][0]);
#pragma unroll
                for (int j = 0; j < 4; ++j) sbv[kk >> 1][j] = b[j];
            }
#define K1_MM(mi, ni) acc[mi][ni] = TRANS ? MFMA32(bfr[c][ni], af[c][mi], acc[mi][ni]) : MFMA32(af[c][mi], bfr[c][ni], acc[mi][ni])
            K1_MM(0, 0);
            if constexpr (ns < 40) af[n][0] = lda(nst, nkk, 0);
            if constexpr (NV >= 1) stat(a[0]);
            SB0();
            K1_MM(0, 1);
            if constexpr (ns < 40) bfr[n][0] = ldb(nst, nkk, 0);
            if constexpr (dm) dma(ds, dj < 2 ? 2 * dj : dj + 2);
            if constexpr (NV >= 4) stat(a[3]);
            SB0();
            K1_MM(1, 0);
            if constexpr (ns < 40) bfr[n][1] = ldb(nst, nkk, 1);
            if constexpr (NV >= 2) stat(a[1]);
            SB0();
            K1_MM(1, 1);
            if constexpr (ns < 40) af[n][1] = lda(nst, nkk, 1);
            if constexpr (dm && dj < 2) dma(ds, 2 * dj + 1);
            if constexpr (NV >= 3) stat(a[2]);
            SB0();
        });
#pragma unroll
        for (int j = NV; j < 4; ++j) { rs2 = dot2(sa[0][j], 0x3f803f80u, rs2); rq2 = dot2(sa[0][j], sa[0][j], rq2); }
    };
    if (is_v) run(std::integral_constant<bool, false>{}); else run(std::integral_constant<bool, true>{});

    float* st_mu = (float*)(smem + 6 * K1_AW);
    float* st_r1 = st_mu + 256;
    float* st_r2 = st_r1 + 256;
    float* st_cs = st_r2 + 256;
    float rs = rs0 + rs1 + rs2, rq = rq0 + rq1 + rq2;
    rs += xhalf(rs); rq += xhalf(rq); cs += xhalf(cs);
    {
        const float mu = rs * (1.0f / DM);
        const float var = __builtin_fmaxf(rq * (1.0f / DM) - mu * mu, 0.f);
        const float r1 = __builtin_amdgcn_rsqf(var + LN_EPS);
        const float r12 = r1 * __builtin_amdgcn_rsqf(var * r1 * r1 + LN_EPS);
        float* pa = hh ? st_r2 + wm * 64 + wn * 32 + r : st_mu + wm * 64 + wn * 32 + r;
        float* pb = hh ? st_cs + (wm >> 1) * 128 + wn * 64 + (wm & 1) * 32 + r : st_r1 + wm * 64 + wn * 32 + r;
        *pa = hh ? r12 : mu;
        *pb = hh ? cs : r1;
    }
    BAR_L();
    if (cb == 28 && wn == 1) return;
    if (!is_mlp) __builtin_amdgcn_s_setprio(3);
    const int n0 = tq * 64;

    if (is_v) {
        f32x4 mu[2][4], sc[2][4];
#pragma unroll
        for (int mi = 0; mi < 2; ++mi)
#pragma unroll
            for (int q4 = 0; q4 < 4; ++q4) {
                mu[mi][q4] = *(const f32x4*)(st_mu + wm * 64 + (mi ^ wn) * 32 + 8 * q4 + 4 * hh);
                sc[mi][q4] = *(const f32x4*)(st_r1 + wm * 64 + (mi ^ wn) * 32 + 8 * q4 + 4 * hh);
            }
#pragma unroll
        for (int ni = 0; ni < 2; ++ni) {
            const int cl = (ni ^ (wm & 1)) * 32 + r;
            const float c = st_cs[wn * 64 + cl] + st_cs[128 + wn * 64 + cl];
            const int nn = n0 - 2 * DM + cl, head = nn / DH, d = nn - head * DH;
#pragma unroll
            for (int mi = 0; mi < 2; ++mi) {
                const int rl = wm * 64 + (mi ^ wn) * 32;
                const int dst = (int)WS_KV + (head * 64 + ((m0 + rl) >> 5)) * KVT_B + 1536 + (hh * 24 + d) * 16;
#pragma unroll
                for (int s = 0; s < 2; ++s) {
                    u32x4 o;
#pragma unroll
                    for (int gq = 0; gq < 2; ++gq) {
                        const f32x4 a = {acc[mi][ni][8 * s + 4 * gq], acc[mi][ni][8 * s + 4 * gq + 1], acc[mi][ni][8 * s + 4 * gq + 2], acc[mi][ni][8 * s + 4 * gq + 3]};
                        const f32x4 v = (a - mu[mi][2 * s + gq] * c) * sc[mi][2 * s + gq];
                        o[2 * gq] = cvtpk(v[0], v[1]); o[2 * gq + 1] = cvtpk(v[2], v[3]);
                    }
                    __builtin_amdgcn_raw_buffer_store_b128(o, rsrc, dst + s * 768, 0, AUX_WT);
                }
            }
        }
    } else {
        const bool isk = tq >= 10;
        float sc[2], cm[2];
#pragma unroll
        for (int mi = 0; mi < 2; ++mi) {
            const int rl = wm * 64 + (mi ^ wn) * 32 + r;
            sc[mi] = is_mlp ? st_r2[rl] : st_r1[rl] * (isk ? 1.0f : QSCALE);
            cm[mi] = -st_mu[rl] * sc[mi];
        }
        f32x4 cc[2][4];
#pragma unroll
        for (int ni = 0; ni < 2; ++ni)
#pragma unroll
            for (int gq = 0; gq < 4; ++gq) {
                const int cl = wn * 64 + (ni ^ (wm & 1)) * 32 + 8 * gq + 4 * hh;
                cc[ni][gq] = *(const f32x4*)(st_cs + cl) + *(const f32x4*)(st_cs + 128 + cl);
            }
        if (is_mlp) {
            float c1 = -2.0f * 0.7978845608028654f * LOG2E, c2 = c1 * 0.044715f;
            asm volatile("" : "+v"(c1), "+v"(c2));
#pragma unroll
            for (int ni = 0; ni < 2; ++ni) {
#pragma unroll
                for (int mi = 0; mi < 2; ++mi) {
                    unsigned pk[8];
#pragma unroll
                    for (int gq = 0; gq < 4; ++gq) {
                        const f32x4 bv = {bf_lo(bb[ni][gq][0]), bf_hi(bb[ni][gq][0]), bf_lo(bb[ni][gq][1]), bf_hi(bb[ni][gq][1])};
                        const f32x4 a = {acc[mi][ni][4 * gq], acc[mi][ni][4 * gq + 1], acc[mi][ni][4 * gq + 2], acc[mi][ni][4 * gq + 3]};
                        const f32x4 u = a * sc[mi] + (cc[ni][gq] * cm[mi] + bv);
                        const f32x4 t = u * (u * u * c2 + c1);
                        f32x4 d;
#pragma unroll
                        for (int e = 0; e < 4; ++e) d[e] = __builtin_amdgcn_exp2f(t[e]);
                        d = d + 1.0f;
#pragma unroll
                        for (int e = 0; e < 4; ++e) d[e] = __builtin_amdgcn_rcpf(d[e]);
                        const f32x4 v = u * d;
                        pk[2 * gq] = cvtpk(v[0], v[1]); pk[2 * gq + 1] = cvtpk(v[2], v[3]);
                    }
                    const int t16 = (m0 + wm * 64 + mi * 32 + r) >> 4;
                    const int dst = (int)WS_H + (t16 * 54 + ((n0 + (ni ^ (wm & 1)) * 32) >> 5)) * 1024 + (hh * 32 + (r & 15)) * 16;
                    __builtin_amdgcn_raw_buffer_store_b128((u32x4){pk[0], pk[1], pk[2], pk[3]}, rsrc, dst, 0, AUX_WT);
                    __builtin_amdgcn_raw_buffer_store_b128((u32x4){pk[4], pk[5], pk[6], pk[7]}, rsrc, dst + 256, 0, AUX_WT);
                }
            }
        } else {
            const int tb = isk ? KVT_B : QT_B;
            const int base = (int)(isk ? WS_KV : WS_Q) + ((m0 + wm * 64) >> 5) * tb;
#pragma unroll
            for (int ni = 0; ni < 2; ++ni)
#pragma unroll
                for (int gq = 0; gq < 4; ++gq) {
                    const int P = ((n0 - (isk ? DM : 0) + (ni ^ (wm & 1)) * 32) >> 2) + 2 * gq + hh;
                    const int head = P / 5, pc = P - head * 5;
                    const bool odd = r & 1;
                    const int off = base + head * 64 * tb + (pc < 4 ? (pc & 1) * 512 + (pc >> 1) * 256 : 1024) + (r & ~1) * 8 + ((r & 1) ^ wn) * tb;
                    unsigned w[2][2];
#pragma unroll
                    for (int mi = 0; mi < 2; ++mi) {
                        const f32x4 a = {acc[mi][ni][4 * gq], acc[mi][ni][4 * gq + 1], acc[mi][ni][4 * gq + 2], acc[mi][ni][4 * gq + 3]};
                        const f32x4 v = a * sc[mi] + cc[ni][gq] * cm[mi];
                        w[mi][0] = cvtpk(v[0], v[1]); w[mi][1] = cvtpk(v[2], v[3]);
                    }
                    const unsigned r0 = __builtin_amdgcn_mov_dpp(odd ? w[0][0] : w[1][0], 0xB1, 0xf, 0xf, true);
                    const unsigned r1 = __builtin_amdgcn_mov_dpp(odd ? w[0][1] : w[1][1], 0xB1, 0xf, 0xf, true);
                    __builtin_amdgcn_raw_buffer_store_b128((u32x4){odd ? r0 : w[0][0], odd ? r1 : w[0][1], odd ? w[1][0] : r0, odd ? w[1][1] : r1},
                                                           rsrc, off, 0, AUX_WT);
                }
        }
    }
}

constexpr int AT_STG = 8 * KVT_B, K2_LDS = 8 * 20 * 1024;
constexpr int GREG = 4 * 1280;
typedef __attribute__((address_space(1))) unsigned gu32;
#define BARRIER() asm volatile("s_barrier" ::: "memory")
DI void mfma16(f32x4& c, bf16x8 a, bf16x8 b, bool first) {
    if (first) asm volatile("v_mfma_f32_16x16x32_bf16 %0, %1, %2, 0" : "=&v"(c) : "v"(a), "v"(b));
    else asm volatile("v_mfma_f32_16x16x32_bf16 %0, %1, %2, %0" : "+v"(c) : "v"(a), "v"(b));
}

__global__ __launch_bounds__(768) void k2_attn(const u16* __restrict__ x, const u16* __restrict__ b2, char* __restrict__ ws,
                                               u16* __restrict__ out) {
    extern __shared__ __attribute__((aligned(16))) char smem[];
    const int tid = threadIdx.x, lane = tid & 63, wid = __builtin_amdgcn_readfirstlane(tid >> 6);
    const int hh = lane >> 5, r = lane & 31;
    const int g8 = blockIdx.x & 7, idx = blockIdx.x >> 3;
    const int head = g8 * 4 + (idx >> 3), qt = (idx & 7) * 8 + wid;
    const int tb = g8 * 4 + (idx >> 3), nb = idx & 7;
    const unsigned lds0 = (unsigned)(uintptr_t)smem;
    const lds_cptr lds = (lds_cptr)smem;
    const unsigned l16 = lane * 16;
    const auto rsrc = __builtin_amdgcn_make_buffer_rsrc(ws, 0, (int)WS_END, 0x00020000);
    gu32* flags = (gu32*)(ws + WS_FLAG);
    f32x4* red = (f32x4*)smem;
    auto rpos = [&](int reg, int i, int u) { return (reg * 20 + i * 5 + u) * 64 + (lane ^ (2 * u + hh)); };
    auto cpos = [&](int row, int ch) { return ((row >> 4) * 5 + (ch >> 1)) * 64 + (ch & 1) * 32 + ((row & 15) ^ ch); };
    if (wid >= 8) {
        const int gw = wid - 8, k0 = gw < 2 ? gw * 14 : gw * 13 + 2;
        const int abase = (int)WS_H + (tb * 4 * 54 + k0) * 1024, wbase = (int)WS_W2F + (nb * 5 * 54 + k0) * 1024;
        const auto rs_t = __builtin_amdgcn_make_buffer_rsrc(ws, 0, gw < 2 ? (int)WS_END : 0, 0x00020000);
        bf16x8 af[2][4], wf[2][5];
        f32x4 acc[4][5];
        auto ld = [&](int j) {
#pragma unroll
            for (int u = 0; u < 5; ++u) {
                wf[j & 1][u] = __builtin_bit_cast(bf16x8, __builtin_amdgcn_raw_buffer_load_b128(j == 13 ? rs_t : rsrc, l16, wbase + (u * 54 + j) * 1024, 0));
                if (u < 4) af[j & 1][u] = __builtin_bit_cast(bf16x8, __builtin_amdgcn_raw_buffer_load_b128(j == 13 ? rs_t : rsrc, l16, abase + (u * 54 + j) * 1024, 0));
            }
            SB0();
        };
        BARRIER();
        ld(0);
        u32x2 bb[5];
#pragma unroll
        for (int u = 0; u < 5; ++u) bb[u] = *(const u32x2*)(b2 + nb * 80 + u * 16 + (lane >> 4) * 4);
#pragma unroll
        for (int j = 0; j < 14; ++j) {
            if ((j & 1) == 0) BARRIER();
            if (j == 12) { BARRIER(); BARRIER(); }
            if (j == 0) {
                f32x4 bv[5];
#pragma unroll
                for (int u = 0; u < 5; ++u) {
                    if (gw) bb[u] = (u32x2){0u, 0u};
                    bv[u] = (f32x4){bf_lo(bb[u][0]), bf_hi(bb[u][0]), bf_lo(bb[u][1]), bf_hi(bb[u][1])};
                }
                asm volatile("s_nop 3" : "+v"(bv[0]), "+v"(bv[1]), "+v"(bv[2]), "+v"(bv[3]), "+v"(bv[4]));
#pragma unroll
                for (int i = 0; i < 4; ++i)
#pragma unroll
                    for (int u = 0; u < 5; ++u)
                        asm volatile("v_mfma_f32_16x16x32_bf16 %0, %1, %2, %3" : "=&v"(acc[i][u]) : "v"(wf[0][u]), "v"(af[0][i]), "v"(bv[u]));
            } else {
#pragma unroll
                for (int i = 0; i < 4; ++i)
#pragma unroll
                    for (int u = 0; u < 5; ++u) mfma16(acc[i][u], wf[j & 1][u], af[j & 1][i], false);
            }
            SB0();
            if (j == 0) ld(1);
            if (j + 2 < 14) ld(j + 2);
        }
        asm volatile("s_nop 15\n\ts_nop 15");
        SB0();
#pragma unroll
        for (int i = 0; i < 4; ++i)
#pragma unroll
            for (int u = 0; u < 5; ++u) red[GREG + rpos(gw, i, u)] = acc[i][u];
        BAR_L();
        f32x4* src = red + GREG + gw * 64 + lane;
        f32x4 pt[5][4];
#pragma unroll
        for (int c = 0; c < 5; ++c)
#pragma unroll
            for (int w = 0; w < 4; ++w) pt[c][w] = src[c * 256 + w * 1280];
        SB0();
#pragma unroll
        for (int c = 0; c < 5; ++c) {
            f32x4 g = (pt[c][0] + pt[c][1]) + (pt[c][2] + pt[c][3]);
#pragma unroll
            for (int e = 0; e < 4; ++e) g[e] = g[e] * __builtin_amdgcn_rcpf(1.0f + __builtin_amdgcn_exp2f(-LOG2E * g[e]));
            src[c * 256 + 1280] = g;
        }
        BAR_L();
        return;
    }
    {
    __builtin_amdgcn_s_setprio(2);
    const char* kvsrc = ws + WS_KV + (size_t)(head * 64 + wid) * KVT_B;
    auto dma = [&](int st, int i) { glds16s(kvsrc + (size_t)st * AT_STG + i * 1024, l16, lds0 + (st % 3) * AT_STG + wid * KVT_B + i * 1024); };
    const char* qsrc = ws + WS_Q + (size_t)(head * 64 + qt) * QT_B;
    u32x2 q1a, q1b, q2u;
    asm volatile("global_load_dwordx2 %0, %1, %2" : "=v"(q1a) : "v"(lane * 8), "s"(qsrc) : "memory");
    asm volatile("global_load_dwordx2 %0, %1, %2 offset:512" : "=v"(q1b) : "v"(lane * 8), "s"(qsrc) : "memory");
    asm volatile("global_load_dwordx2 %0, %1, %2 offset:1024" : "=v"(q2u) : "v"(r * 8), "s"(qsrc) : "memory");
#pragma unroll
    for (int st = 0; st < 3; ++st) {
#pragma unroll
        for (int i = 0; i < 3; ++i) dma(st, i);
        if (st == 0) BARRIER();
    }

    const int mh = wid & 1, kq = wid >> 1;
    const int abase = (int)WS_O + ((tb * 4 + mh * 2) * 20 + kq * 5) * 1024, wbase = (int)WS_WOF + (nb * 5 * 20 + kq * 5) * 1024;
    bf16x8 af[5][2], wf[4][5];
    f32x4 acc[2][5];
    u32x4 xx[2];
    auto ldw1 = [&](int j, int u) {
        wf[j & 3][u] = __builtin_bit_cast(bf16x8, __builtin_amdgcn_raw_buffer_load_b128(rsrc, l16, wbase + (u * 20 + j) * 1024, 0));
    };
    auto ldw = [&](int j) {
#pragma unroll
        for (int u = 0; u < 5; ++u) ldw1(j, u);
    };
    const int reff = r < 21 ? r : 21;
    const lds_cptr k1p = lds + lane * 8, k2p = lds + 1024 + lane * 8, vp = lds + 1536 + (hh * 24 + reff) * 16;
    auto toff = [](int T) { return ((T >> 3) % 3) * AT_STG + (T & 7) * KVT_B; };
    auto ldk1 = [&](int T) { return cat8(lds64(k1p + toff(T)), lds64(k1p + toff(T) + 512)); };
    f32x16 zero16;
#pragma unroll
    for (int e = 0; e < 16; ++e) zero16[e] = 0.f;
    f32x16 o = zero16;

    auto set_m = [&](float m) {
        const unsigned a = cvtpk(-m, 0.f) & 0xffffu;
        const float r1 = -m - bf_lo(a);
        const unsigned b = cvtpk(r1, 0.f) & 0xffffu;
        const float r2 = r1 - bf_lo(b);
        const unsigned c = cvtpk(r2, 0.f) & 0xffffu;
        if (hh) q2u = (u32x2){a | (b << 16), c};
    };

    asm volatile("s_waitcnt vmcnt(6)\n\ts_barrier" : "+v"(q1a), "+v"(q1b), "+v"(q2u) :: "memory");
    if (hh) q2u = (u32x2){0u, 0u};
    const bf16x8 q1 = __builtin_bit_cast(bf16x8, (u32x4){q1a[0], q1a[1], q1b[0], q1b[1]});
    {
        f32x16 s = MFMA32(ldk1(0), q1, zero16);
        s = MFMA32K8(lds64(k2p), __builtin_bit_cast(bf16x4, q2u), s);
        float m = s[0];
#pragma unroll
        for (int e = 1; e < 16; ++e) m = __builtin_fmaxf(m, s[e]);
        m = __builtin_fmaxf(m, xhalf(m));
        set_m(m);
    }
    {
        const bf16x4 q2 = __builtin_bit_cast(bf16x4, q2u);
        f32x16 sb[2];
        sb[0] = MFMA32(ldk1(0), q1, zero16);
        sb[0] = MFMA32K8(lds64(k2p), q2, sb[0]);
        bf16x8 k1 = ldk1(1), v0 = lds128(vp), v1;
        bf16x4 k2 = lds64(k2p + toff(1));
        u32x4 p0, p1;
        SB0();
        static_for<256>([&](auto GI) {
            constexpr int g = decltype(GI)::value, T = g >> 2, sl = g & 3, st = T >> 3;
            constexpr int c = T & 1, n = c ^ 1;
            if constexpr ((T & 7) == 6 && sl == 0 && st < 7) {
                if constexpr (st == 0) asm volatile("s_waitcnt vmcnt(3)\n\ts_barrier" ::: "memory");
                else asm volatile("s_waitcnt vmcnt(0)\n\ts_barrier" ::: "memory");
            }
            if constexpr (sl == 0) {
                if constexpr (T < 63) sb[n] = MFMA32(k1, q1, zero16);
                if constexpr (T < 62) k1 = ldk1(T + 2);
            } else if constexpr (sl == 1) {
                if constexpr (T > 0) o = MFMA32(v1, __builtin_bit_cast(bf16x8, p1), o);
                v1 = lds128(vp + toff(T) + 768);
            } else if constexpr (sl == 2) {
                if constexpr (T < 63) sb[n] = MFMA32K8(k2, q2, sb[n]);
                if constexpr (T < 62) k2 = lds64(k2p + toff(T + 2));
            } else {
                o = MFMA32(v0, __builtin_bit_cast(bf16x8, p0), o);
                if constexpr (T < 63) v0 = lds128(vp + toff(T + 1));
            }
            if constexpr ((T & 7) == 6 && sl < 3 && st >= 1 && st + 2 <= 7) dma(st + 2, sl);
            if constexpr (g >= 218 && g < 218 + 5 * 3) ldw1((g - 218) / 5, (g - 218) % 5);
            SB0();
            const float e0 = __builtin_amdgcn_exp2f(sb[c][4 * sl]), e1 = __builtin_amdgcn_exp2f(sb[c][4 * sl + 1]);
            const float e2 = __builtin_amdgcn_exp2f(sb[c][4 * sl + 2]), e3 = __builtin_amdgcn_exp2f(sb[c][4 * sl + 3]);
            if constexpr (sl < 2) { p0[2 * sl] = cvtpk(e0, e1); p0[2 * sl + 1] = cvtpk(e2, e3); }
            else { p1[2 * sl - 4] = cvtpk(e0, e1); p1[2 * sl - 3] = cvtpk(e2, e3); }
            SB0();
        });
        o = MFMA32(v1, __builtin_bit_cast(bf16x8, p1), o);
    }
    float l = o[8];
    { const float lx = xhalf(l); l = hh ? l : lx; }
    bool bad = !(l < 1.0e18f) || !(l > 0.25f);
#pragma unroll
    for (int e = 0; e < 12; ++e) bad |= !(__builtin_fabsf(o[e]) < 3.0e38f) && (e < 8 || !hh);
    if (__builtin_amdgcn_ballot_w64(bad) != 0) {
        const char* kvh = ws + WS_KV + (size_t)head * 64 * KVT_B;
        for (int pass = 0; pass < 2; ++pass) {
            if (pass == 0 && hh) q2u = (u32x2){0u, 0u};
            float m = -INFINITY;
            o = zero16;
            for (int T = 0; T < 64; ++T) {
                const char* kt = kvh + T * KVT_B;
                f32x16 s = MFMA32(cat8(*(const bf16x4*)(kt + lane * 8), *(const bf16x4*)(kt + 512 + lane * 8)), q1, zero16);
                s = MFMA32K8(*(const bf16x4*)(kt + 1024 + lane * 8), __builtin_bit_cast(bf16x4, q2u), s);
                if (pass == 0) {
#pragma unroll
                    for (int e = 0; e < 16; ++e) m = __builtin_fmaxf(m, s[e]);
                } else {
                    u32x4 p0, p1;
#pragma unroll
                    for (int e = 0; e < 4; ++e) {
                        p0[e] = cvtpk(__builtin_amdgcn_exp2f(s[2 * e]), __builtin_amdgcn_exp2f(s[2 * e + 1]));
                        p1[e] = cvtpk(__builtin_amdgcn_exp2f(s[8 + 2 * e]), __builtin_amdgcn_exp2f(s[9 + 2 * e]));
                    }
                    const char* vt = kt + 1536 + (hh * 24 + reff) * 16;
                    o = MFMA32(*(const bf16x8*)vt, __builtin_bit_cast(bf16x8, p0), o);
                    o = MFMA32(*(const bf16x8*)(vt + 768), __builtin_bit_cast(bf16x8, p1), o);
                }
            }
            if (pass == 0) { m = __builtin_fmaxf(m, xhalf(m)); set_m(m); }
        }
        l = o[8];
        { const float lx = xhalf(l); l = hh ? l : lx; }
    }
    const float il = 1.0f / l;
    const int obase = (int)WS_O + (qt * 2 + (r >> 4)) * 20 * 1024 + (r & 15) * 16;
#pragma unroll
    for (int i = 0; i < 3; ++i) {
        if (i < 2 || hh == 0) {
            const int G = 5 * head + 2 * i + hh;
            __builtin_amdgcn_raw_buffer_store_b64((u32x2){cvtpk(o[4 * i] * il, o[4 * i + 1] * il), cvtpk(o[4 * i + 2] * il, o[4 * i + 3] * il)},
                                                  rsrc, obase + (G >> 3) * 1024 + ((G >> 1) & 3) * 256 + (G & 1) * 8, 0, AUX_WT);
        }
    }
    WAITV_BAR(0);
    if (tid == 448) __hip_atomic_store(flags + (idx & 7) * 64 + head, 1u, __ATOMIC_RELAXED, __HIP_MEMORY_SCOPE_AGENT);
#pragma unroll
    for (int j = 3; j < 4; ++j) ldw(j);
    SB0();
    BARRIER();
    for (unsigned spin = 0; spin < (1u << 22); ++spin) {
        const unsigned f = __hip_atomic_load(flags + g8 * 64 + kq * 8 + (lane & 7), __ATOMIC_RELAXED, __HIP_MEMORY_SCOPE_AGENT);
        if (__builtin_amdgcn_ballot_w64(f == 1u) == ~0ull) break;
        __builtin_amdgcn_s_sleep(2);
    }
    __builtin_amdgcn_fence(__ATOMIC_ACQUIRE, "wavefront");
#pragma unroll
    for (int j = 0; j < 5; ++j)
#pragma unroll
        for (int i = 0; i < 2; ++i)
            af[j][i] = __builtin_bit_cast(bf16x8, __builtin_amdgcn_raw_buffer_load_b128(rsrc, l16, abase + (i * 20 + j) * 1024, AUX_WT));
    SB0();
#pragma unroll
    for (int j = 0; j < 5; ++j) {
#pragma unroll
        for (int i = 0; i < 2; ++i)
#pragma unroll
            for (int u = 0; u < 5; ++u) mfma16(acc[i][u], wf[j & 3][u], af[j][i], j == 0);
        SB0();
        if (j == 0) { ldw(4); SB0(); }
        if (j == 1) {
#pragma unroll
            for (int ps = 0; ps < 2; ++ps) {
                const int c = (wid < 2 ? ps * 512 : 0) + wid * 64 + lane_id(), row = c / 10, ch = c - row * 10;
                xx[ps] = *(const u32x4*)(x + (size_t)(tb * 64 + row) * DM + nb * 80 + ch * 8);
            }
            SB0();
        }
    }
    asm volatile("s_nop 15\n\ts_nop 15");
    SB0();
#pragma unroll
    for (int i = 0; i < 2; ++i)
#pragma unroll
        for (int u = 0; u < 5; ++u) red[rpos(kq, mh * 2 + i, u)] = acc[i][u];
    BAR_L();
#pragma unroll
    for (int ps = 0; ps < 2; ++ps) {
        if (ps == 0 || wid < 2) {
            const int c = ps * 512 + tid, row = c / 10, ch = c - row * 10;
            const f32x4* src = red + cpos(row, ch);
            const f32x4 s0 = (src[0] + src[1280]) + (src[2560] + src[3840]), s1 = (src[16] + src[1296]) + (src[2576] + src[3856]);
            const f32x4 g0 = src[GREG + 1280], g1 = src[GREG + 1296];
            const u32x4 xv = xx[ps];
            u32x4 ov;
            ov[0] = cvtpk(__builtin_fmaf(s0[0], g0[0], bf_lo(xv[0])), __builtin_fmaf(s0[1], g0[1], bf_hi(xv[0])));
            ov[1] = cvtpk(__builtin_fmaf(s0[2], g0[2], bf_lo(xv[1])), __builtin_fmaf(s0[3], g0[3], bf_hi(xv[1])));
            ov[2] = cvtpk(__builtin_fmaf(s1[0], g1[0], bf_lo(xv[2])), __builtin_fmaf(s1[1], g1[1], bf_hi(xv[2])));
            ov[3] = cvtpk(__builtin_fmaf(s1[2], g1[2], bf_lo(xv[3])), __builtin_fmaf(s1[3], g1[3], bf_hi(xv[3])));
            *(u32x4*)(out + (size_t)(tb * 64 + row) * DM + nb * 80 + ch * 8) = ov;
        }
    }
    }
}

extern "C" void kernel_launch(void* const* d_in, const int* in_sizes, int n_in, void* d_out, int out_size, void* d_ws, size_t ws_size,
                              hipStream_t stream) {
    const u16* x = (const u16*)d_in[0];
    const u16* wqkv = (const u16*)d_in[1];
    const u16* wo = (const u16*)d_in[2];
    const u16* w1 = (const u16*)d_in[4];
    const u16* b1 = (const u16*)d_in[5];
    const u16* w2 = (const u16*)d_in[6];
    const u16* b2 = (const u16*)d_in[7];
    char* ws = (char*)d_ws;
    (void)hipFuncSetAttribute((const void*)k1_proj, hipFuncAttributeMaxDynamicSharedMemorySize, K1_LDS);
    (void)hipFuncSetAttribute((const void*)k2_attn, hipFuncAttributeMaxDynamicSharedMemorySize, K2_LDS);
    k1_proj<<<256, 512, K1_LDS, stream>>>(x, wqkv, w1, b1, w2, wo, ws);
    k2_attn<<<256, 768, K2_LDS, stream>>>(x, b2, ws, (u16*)d_out);
}
```

```cpp
#include <hip/hip_runtime.h>
#include <hip/hip_bf16.h>
#include <stdint.h>
#include <type_traits>

typedef short bf16x8 __attribute__((ext_vector_type(8)));
typedef short bf16x4 __attribute__((ext_vector_type(4)));
typedef float f32x16 __attribute__((ext_vector_type(16)));
typedef float f32x4 __attribute__((ext_vector_type(4)));
typedef unsigned u32x4 __attribute__((ext_vector_type(4)));
typedef unsigned u32x2 __attribute__((ext_vector_type(2)));
typedef __bf16 bf2_t __attribute__((ext_vector_type(2)));
typedef short v4i16_t __attribute__((ext_vector_type(4)));
typedef unsigned short u16;
#define LDS_AS __attribute__((address_space(3)))
typedef LDS_AS const char* lds_cptr;
typedef LDS_AS char* lds_ptr;
#define DI __device__ __forceinline__

constexpr int SEQ = 2048, DM = 640, NH = 32, DH = 20, FF = 1728;
constexpr float LN_EPS = 1e-5f;
constexpr float LOG2E = 1.4426950408889634f;
constexpr float QSCALE = 0.22360679774997896f * LOG2E;

constexpr int QT_B = 1280, KVT_B = 3072;
constexpr size_t WS_Q = 0;
constexpr size_t WS_KV = WS_Q + (size_t)NH * 64 * QT_B;
constexpr size_t WS_H = WS_KV + (size_t)NH * 64 * KVT_B;
constexpr size_t WS_O = WS_H + (size_t)128 * 54 * 1024;
constexpr size_t WS_W2F = WS_O + (size_t)128 * 20 * 1024;
constexpr size_t WS_WOF = WS_W2F + (size_t)40 * 54 * 1024;
constexpr size_t WS_END = WS_WOF + (size_t)40 * 20 * 1024;
constexpr unsigned EMPTY = 0xffffffffu;
constexpr int AUX_WT = 16;

#define MFMA32(a, b, c) __builtin_amdgcn_mfma_f32_32x32x16_bf16((a), (b), (c), 0, 0, 0)
#define MFMA32K8(a, b, c) __builtin_amdgcn_mfma_f32_32x32x8bf16_1k((a), (b), (c), 0, 0, 0)

typedef float f32x2 __attribute__((ext_vector_type(2)));
DI unsigned cvtpk(float lo, float hi) { return __builtin_bit_cast(unsigned, __builtin_convertvector((f32x2){lo, hi}, bf2_t)); }
DI float bf_lo(unsigned u) { return __uint_as_float(u << 16); }
DI float bf_hi(unsigned u) { return __uint_as_float(u & 0xffff0000u); }
DI float dot2(unsigned a, unsigned b, float c) { return __builtin_amdgcn_fdot2_f32_bf16(__builtin_bit_cast(bf2_t, a), __builtin_bit_cast(bf2_t, b), c, false); }
DI bf16x8 lds128(lds_cptr p) { return *(const LDS_AS bf16x8*)p; }
DI bf16x4 lds64(lds_cptr p) { return *(const LDS_AS bf16x4*)p; }
DI bf16x4 ldstr(lds_cptr p) { return __builtin_bit_cast(bf16x4, __builtin_amdgcn_ds_read_tr16_b64_v4i16((LDS_AS v4i16_t*)p)); }
DI bf16x8 cat8(bf16x4 a, bf16x4 b) { return __builtin_shufflevector(a, b, 0, 1, 2, 3, 4, 5, 6, 7); }
DI float xhalf(float v) {
    auto r = __builtin_amdgcn_permlane32_swap(__float_as_uint(v), __float_as_uint(v), false, false);
    return (threadIdx.x & 32) ? __uint_as_float(r[0]) : __uint_as_float(r[1]);
}
#define WAITV(n) asm volatile("s_waitcnt vmcnt(" #n ")" ::: "memory")
#define WAITV_BAR(n) asm volatile("s_waitcnt vmcnt(" #n ") lgkmcnt(0)\n\ts_barrier" ::: "memory")
#define BAR_L() asm volatile("s_waitcnt lgkmcnt(0)\n\ts_barrier" ::: "memory")

constexpr int K1_AW = 3 * 8192;
constexpr int K1_BOFF = 4 * K1_AW, K1_LDS = 6 * K1_AW + 4096;

template <int N, class F> DI void static_for(F&& f) {
    if constexpr (N > 0) { static_for<N - 1>(f); f(std::integral_constant<int, N - 1>{}); }
}
DI void glds16s(const void* sbase, unsigned voff, unsigned lds_dst) {
    asm volatile("s_mov_b32 m0, %2\n\ts_nop 0\n\tglobal_load_lds_dwordx4 %0, %1" :: "v"(voff), "s"(sbase), "s"(lds_dst) : "memory");
}
DI int lane_id() {
    int l;
    asm volatile("v_mbcnt_lo_u32_b32 %0, -1, 0\n\tv_mbcnt_hi_u32_b32 %0, -1, %0" : "=v"(l));
    return l;
}
DI float vadd(float a, float b) { float d; asm("v_add_f32 %0, %1, %2" : "=v"(d) : "v"(a), "v"(b)); return d; }
DI float vfma(float a, float b, float c) { float d; asm("v_fma_f32 %0, %1, %2, %3" : "=v"(d) : "v"(a), "v"(b), "v"(c)); return d; }
#define SB0() __builtin_amdgcn_sched_barrier(0)

__global__ __launch_bounds__(512) void k1_proj(const u16* __restrict__ x, const u16* __restrict__ wqkv, const u16* __restrict__ w1,
                                               const u16* __restrict__ b1, const u16* __restrict__ w2, const u16* __restrict__ wo,
                                               char* __restrict__ ws) {
    extern __shared__ __attribute__((aligned(16))) char smem[];
    const int tid = threadIdx.x, lane = tid & 63, wid = __builtin_amdgcn_readfirstlane(tid >> 6);
    const int g8 = blockIdx.x & 7, idx = blockIdx.x >> 3;
    const int nq = g8 >> 1, pi = idx >> 2;
    const auto rsrc = __builtin_amdgcn_make_buffer_rsrc(ws, 0, (int)WS_END, 0x00020000);
    if (pi >= (nq == 3 ? 8 : 7)) {
        const int wv = (g8 * 4 + (idx & 3)) * 8 + wid;
        const unsigned ldsw = (unsigned)(uintptr_t)smem + wid * 10240;
        const int g = lane >> 4, q = (lane & 15) >> 2, p = lane & 3;
        for (int un = wv; un < 296; un += 192) {
            const int ksg = un >> 2, qr = un & 3;
            const bool is2 = ksg < 54;
            const int ks = is2 ? ksg : ksg - 54, nks = is2 ? 54 : 20;
            const u16* src = (is2 ? w2 : wo) + (size_t)ks * 32 * DM + qr * 160;
#pragma unroll
            for (int i = 0; i < 10; ++i) {
                const int o = i * 64 + lane, row = o / 20, ch = o - row * 20;
                glds16s(src, row * (DM * 2) + ch * 16, ldsw + i * 1024);
            }
            const lds_cptr tp = (lds_cptr)smem + wid * 10240 + ((is2 ? 16 * (g & 1) + 4 * (g >> 1) : 8 * g) + q) * 320 + p * 8;
            const int second = is2 ? 8 * 320 : 4 * 320;
            WAITV(0);
            const int dst = (int)(is2 ? WS_W2F : WS_WOF) + (qr * 10 * nks + ks) * 1024 + lane * 16;
#pragma unroll
            for (int u = 0; u < 10; ++u)
                __builtin_amdgcn_raw_buffer_store_b128(__builtin_bit_cast(u32x4, cat8(ldstr(tp + u * 32), ldstr(tp + u * 32 + second))),
                                                       rsrc, dst + u * nks * 1024, 0, AUX_WT);
            asm volatile("s_waitcnt lgkmcnt(0)" ::: "memory");
        }
        for (int i = wv * 64 + lane; i < 2048 * 24; i += 192 * 64) {
            const int tile = i / 24, c = i - tile * 24;
            const int off = c < 16 ? 1280 + c * 16 : 1536 + (((c - 16) >> 1) * 24 + 20 + (c & 1)) * 16;
            const unsigned v0 = c < 16 || !(c & 1) ? 0x3f803f80u : 0u, v1 = c < 16 ? 0x00003f80u : v0;
            __builtin_amdgcn_raw_buffer_store_b128((u32x4){v0, v1, v0, v1}, rsrc, (int)WS_KV + tile * KVT_B + off, 0, AUX_WT);
        }
        for (int i = wv; i < 128 * 20; i += 192) {
            __builtin_amdgcn_raw_buffer_store_b128((u32x4){EMPTY, EMPTY, EMPTY, EMPTY}, rsrc, (int)WS_O + i * 1024 + lane * 16, 0, AUX_WT);
            __builtin_amdgcn_s_sleep(12);
        }
        return;
    }
    const int mt = (g8 & 1) * 4 + (idx & 3), cb = nq * 7 + pi;
    const int m0 = mt * 256;
    const bool mlp0 = cb < 27;
    const int t0 = mlp0 ? cb : (cb == 27 ? 27 : 29), t1 = mlp0 ? cb : (cb == 27 ? 28 : 29);
    const u16* B0 = (mlp0 ? w1 : wqkv) + t0 * 64;
    const u16* B1 = wqkv + t1 * 64;
    const int ld0 = mlp0 ? FF : 3 * DM;
    const u16* A0 = x + (size_t)m0 * DM;
    unsigned avo[4], bvo[2];
#pragma unroll
    for (int i = 0; i < 4; ++i) {
        const int row = wid * 32 + i * 8 + (lane >> 3);
        avo[i] = row * (DM * 2) + (((lane & 7) ^ ((row >> 1) & 7)) << 4);
    }
    {
        const int row = wid * 8 + (lane >> 3), ch = (lane & 7) ^ (((row >> 1) & 1) << 2);
        bvo[0] = row * ld0 * 2 + ch * 16;
        bvo[1] = row * (3 * DM * 2) + ch * 16;
    }
    const unsigned lds0 = (unsigned)(uintptr_t)smem;
    const lds_cptr lds = (lds_cptr)smem;
    const unsigned adst = lds0 + (wid >> 1) * K1_AW + (wid & 1) * 4096, bdst = lds0 + K1_BOFF + wid * 1024;
    auto dma = [&](int st, int j) {
        const int slot = st % 3;
        if (j < 4) glds16s(A0 + st * 64, avo[j], adst + slot * 8192 + j * 1024);
        else if (j == 4) glds16s(B0 + (size_t)st * 64 * ld0, bvo[0], bdst + slot * 8192);
        else glds16s(B1 + (size_t)st * 64 * (3 * DM), bvo[1], bdst + K1_AW + slot * 8192);
    };
#pragma unroll
    for (int st = 0; st < 3; ++st)
#pragma unroll
        for (int j = 0; j < 6; ++j) dma(st, j);

    const int wm = wid & 3, wn = wid >> 2, hh = lane >> 5, r = lane & 31;
    const int tq = wn ? t1 : t0;
    const bool is_mlp = mlp0 && wn == 0;
    const bool is_v = !is_mlp && tq >= 20;
    u32x2 bb[2][4];
#pragma unroll
    for (int ni = 0; ni < 2; ++ni)
#pragma unroll
        for (int gq = 0; gq < 4; ++gq) bb[ni][gq] = *(const u32x2*)(b1 + (mlp0 ? t0 * 64 : 0) + (ni ^ (wm & 1)) * 32 + 8 * gq + 4 * hh);
    f32x16 acc[2][2];
#pragma unroll
    for (int i = 0; i < 2; ++i)
#pragma unroll
        for (int j = 0; j < 2; ++j)
#pragma unroll
            for (int e = 0; e < 16; ++e) acc[i][j][e] = 0.f;
    float rs0 = 0.f, rs1 = 0.f, rq0 = 0.f, rq1 = 0.f, rs2 = 0.f, rq2 = 0.f, cs = 0.f;

    auto run = [&](auto TR) {
        constexpr bool TRANS = decltype(TR)::value;
        const int par = wm >> 1;
        lds_cptr ap[4][2];
#pragma unroll
        for (int kk = 0; kk < 4; ++kk)
#pragma unroll
            for (int mi = 0; mi < 2; ++mi)
                ap[kk][mi] = lds + wm * K1_AW + ((mi ^ wn) * 32 + r) * 128 + (((2 * (kk ^ par) + hh) ^ ((r >> 1) & 7)) << 4);
        const int g = lane >> 4, q = (lane & 15) >> 2, p = lane & 3;
        lds_cptr bp[2][2];
#pragma unroll
        for (int ni = 0; ni < 2; ++ni)
#pragma unroll
            for (int k1 = 0; k1 < 2; ++k1)
                bp[ni][k1] = lds + K1_BOFF + wn * K1_AW + (k1 ^ par) * 2048 + (8 * (g >> 1) + q) * 128 +
                             (((((ni ^ wm ^ (q >> 1)) & 1) << 2) + 2 * (g & 1) + (p >> 1)) << 4) + (p & 1) * 8;
        bf16x8 af[2][2], bfr[2][2];
        auto lda = [&](int st, int kk, int mi) { return lds128(ap[kk][mi] + (st % 3) * 8192); };
        auto ldb = [&](int st, int kk, int ni) {
            const lds_cptr s = bp[ni][kk & 1] + (st % 3) * 8192 + (kk & 2) * 2048;
            return cat8(ldstr(s), ldstr(s + 512));
        };
        auto stat = [&](unsigned u) {
            const float lo = bf_lo(u), hi = bf_hi(u);
            rs0 = vadd(rs0, lo); rq0 = vfma(lo, lo, rq0);
            rs1 = vadd(rs1, hi); rq1 = vfma(hi, hi, rq1);
        };
        WAITV_BAR(20);
        af[0][0] = lda(0, 0, 0); af[0][1] = lda(0, 0, 1); bfr[0][0] = ldb(0, 0, 0); bfr[0][1] = ldb(0, 0, 1);
        constexpr int NV = 3;
        unsigned sa[4][4], sbv[2][4];
        auto clump = [&](int first) {
#pragma unroll
            for (int k = first; k < 4; ++k)
#pragma unroll
                for (int j = NV; j < 4; ++j) { rs2 = dot2(sa[k][j], 0x3f803f80u, rs2); rq2 = dot2(sa[k][j], sa[k][j], rq2); }
#pragma unroll
            for (int k = 0; k < 2; ++k)
#pragma unroll
                for (int j = 0; j < 4; ++j) cs = dot2(sbv[k][j], 0x3f803f80u, cs);
            SB0();
        };
        static_for<40>([&](auto S) {
            constexpr int s = decltype(S)::value, st = s >> 2, kk = s & 3, c = s & 1, n = c ^ 1;
            constexpr int ns = s + 1, nst = ns >> 2, nkk = ns & 3;
            if constexpr (kk == 3) clump(st == 0 ? 1 : 0);
            if constexpr (kk == 3 && st < 9) {
                if constexpr (st == 0) { WAITV_BAR(14); } else
                if constexpr (st < 8) { WAITV_BAR(6); } else { WAITV_BAR(0); }
            }
            constexpr int ds = (s + 1) / 4 + 2, dj = (s + 1) & 3;
            constexpr bool dm = s >= 3 && ds < 10;
            const u32x4 a = __builtin_bit_cast(u32x4, af[c][0]);
#pragma unroll
            for (int j = 0; j < 4; ++j) sa[(kk + 1) & 3][j] = a[j];
            if constexpr ((kk & 1) == 0) {
                const u32x4 b = __builtin_bit_cast(u32x4, bfr[c][0]);
#pragma unroll
                for (int j = 0; j < 4; ++j) sbv[kk >> 1][j] = b[j];
            }
#define K1_MM(mi, ni) acc[mi][ni] = TRANS ? MFMA32(bfr[c][ni], af[c][mi], acc[mi][ni]) : MFMA32(af[c][mi], bfr[c][ni], acc[mi][ni])
            K1_MM(0, 0);
            if constexpr (ns < 40) af[n][0] = lda(nst, nkk, 0);
            if constexpr (NV >= 1) stat(a[0]);
            SB0();
            K1_MM(0, 1);
            if constexpr (ns < 40) bfr[n][0] = ldb(nst, nkk, 0);
            if constexpr (dm) dma(ds, dj < 2 ? 2 * dj : dj + 2);
            if constexpr (NV >= 4) stat(a[3]);
            SB0();
            K1_MM(1, 0);
            if constexpr (ns < 40) bfr[n][1] = ldb(nst, nkk, 1);
            if constexpr (NV >= 2) stat(a[1]);
            SB0();
            K1_MM(1, 1);
            if constexpr (ns < 40) af[n][1] = lda(nst, nkk, 1);
            if constexpr (dm && dj < 2) dma(ds, 2 * dj + 1);
            if constexpr (NV >= 3) stat(a[2]);
            SB0();
        });
#pragma unroll
        for (int j = NV; j < 4; ++j) { rs2 = dot2(sa[0][j], 0x3f803f80u, rs2); rq2 = dot2(sa[0][j], sa[0][j], rq2); }
    };
    if (is_v) run(std::integral_constant<bool, false>{}); else run(std::integral_constant<bool, true>{});

    float* st_mu = (float*)(smem + 6 * K1_AW);
    float* st_r1 = st_mu + 256;
    float* st_r2 = st_r1 + 256;
    float* st_cs = st_r2 + 256;
    float rs = rs0 + rs1 + rs2, rq = rq0 + rq1 + rq2;
    rs += xhalf(rs); rq += xhalf(rq); cs += xhalf(cs);
    {
        const float mu = rs * (1.0f / DM);
        const float var = __builtin_fmaxf(rq * (1.0f / DM) - mu * mu, 0.f);
        const float r1 = __builtin_amdgcn_rsqf(var + LN_EPS);
        const float r12 = r1 * __builtin_amdgcn_rsqf(var * r1 * r1 + LN_EPS);
        float* pa = hh ? st_r2 + wm * 64 + wn * 32 + r : st_mu + wm * 64 + wn * 32 + r;
        float* pb = hh ? st_cs + (wm >> 1) * 128 + wn * 64 + (wm & 1) * 32 + r : st_r1 + wm * 64 + wn * 32 + r;
        *pa = hh ? r12 : mu;
        *pb = hh ? cs : r1;
    }
    BAR_L();
    if (cb == 28 && wn == 1) return;
    if (!is_mlp) __builtin_amdgcn_s_setprio(3);
    const int n0 = tq * 64;

    if (is_v) {
        f32x4 mu[2][4], sc[2][4];
#pragma unroll
        for (int mi = 0; mi < 2; ++mi)
#pragma unroll
            for (int q4 = 0; q4 < 4; ++q4) {
                mu[mi][q4] = *(const f32x4*)(st_mu + wm * 64 + (mi ^ wn) * 32 + 8 * q4 + 4 * hh);
                sc[mi][q4] = *(const f32x4*)(st_r1 + wm * 64 + (mi ^ wn) * 32 + 8 * q4 + 4 * hh);
            }
#pragma unroll
        for (int ni = 0; ni < 2; ++ni) {
            const int cl = (ni ^ (wm & 1)) * 32 + r;
            const float c = st_cs[wn * 64 + cl] + st_cs[128 + wn * 64 + cl];
            const int nn = n0 - 2 * DM + cl, head = nn / DH, d = nn - head * DH;
#pragma unroll
            for (int mi = 0; mi < 2; ++mi) {
                const int rl = wm * 64 + (mi ^ wn) * 32;
                const int dst = (int)WS_KV + (head * 64 + ((m0 + rl) >> 5)) * KVT_B + 1536 + (hh * 24 + d) * 16;
#pragma unroll
                for (int s = 0; s < 2; ++s) {
                    u32x4 o;
#pragma unroll
                    for (int gq = 0; gq < 2; ++gq) {
                        const f32x4 a = {acc[mi][ni][8 * s + 4 * gq], acc[mi][ni][8 * s + 4 * gq + 1], acc[mi][ni][8 * s + 4 * gq + 2], acc[mi][ni][8 * s + 4 * gq + 3]};
                        const f32x4 v = (a - mu[mi][2 * s + gq] * c) * sc[mi][2 * s + gq];
                        o[2 * gq] = cvtpk(v[0], v[1]); o[2 * gq + 1] = cvtpk(v[2], v[3]);
                    }
                    __builtin_amdgcn_raw_buffer_store_b128(o, rsrc, dst + s * 768, 0, AUX_WT);
                }
            }
        }
    } else {
        const bool isk = tq >= 10;
        float sc[2], cm[2];
#pragma unroll
        for (int mi = 0; mi < 2; ++mi) {
            const int rl = wm * 64 + (mi ^ wn) * 32 + r;
            sc[mi] = is_mlp ? st_r2[rl] : st_r1[rl] * (isk ? 1.0f : QSCALE);
            cm[mi] = -st_mu[rl] * sc[mi];
        }
        f32x4 cc[2][4];
#pragma unroll
        for (int ni = 0; ni < 2; ++ni)
#pragma unroll
            for (int gq = 0; gq < 4; ++gq) {
                const int cl = wn * 64 + (ni ^ (wm & 1)) * 32 + 8 * gq + 4 * hh;
                cc[ni][gq] = *(const f32x4*)(st_cs + cl) + *(const f32x4*)(st_cs + 128 + cl);
            }
        if (is_mlp) {
            float c1 = -2.0f * 0.7978845608028654f * LOG2E, c2 = c1 * 0.044715f;
            asm volatile("" : "+v"(c1), "+v"(c2));
#pragma unroll
            for (int ni = 0; ni < 2; ++ni) {
#pragma unroll
                for (int mi = 0; mi < 2; ++mi) {
                    unsigned pk[8];
#pragma unroll
                    for (int gq = 0; gq < 4; ++gq) {
                        const f32x4 bv = {bf_lo(bb[ni][gq][0]), bf_hi(bb[ni][gq][0]), bf_lo(bb[ni][gq][1]), bf_hi(bb[ni][gq][1])};
                        const f32x4 a = {acc[mi][ni][4 * gq], acc[mi][ni][4 * gq + 1], acc[mi][ni][4 * gq + 2], acc[mi][ni][4 * gq + 3]};
                        const f32x4 u = a * sc[mi] + (cc[ni][gq] * cm[mi] + bv);
                        const f32x4 t = u * (u * u * c2 + c1);
                        f32x4 d;
#pragma unroll
                        for (int e = 0; e < 4; ++e) d[e] = __builtin_amdgcn_exp2f(t[e]);
                        d = d + 1.0f;
#pragma unroll
                        for (int e = 0; e < 4; ++e) d[e] = __builtin_amdgcn_rcpf(d[e]);
                        const f32x4 v = u * d;
                        pk[2 * gq] = cvtpk(v[0], v[1]); pk[2 * gq + 1] = cvtpk(v[2], v[3]);
                    }
                    const int t16 = (m0 + wm * 64 + mi * 32 + r) >> 4;
                    const int dst = (int)WS_H + (t16 * 54 + ((n0 + (ni ^ (wm & 1)) * 32) >> 5)) * 1024 + (hh * 32 + (r & 15)) * 16;
                    __builtin_amdgcn_raw_buffer_store_b128((u32x4){pk[0], pk[1], pk[2], pk[3]}, rsrc, dst, 0, AUX_WT);
                    __builtin_amdgcn_raw_buffer_store_b128((u32x4){pk[4], pk[5], pk[6], pk[7]}, rsrc, dst + 256, 0, AUX_WT);
                }
            }
        } else {
            const int tb = isk ? KVT_B : QT_B;
            const int base = (int)(isk ? WS_KV : WS_Q) + ((m0 + wm * 64) >> 5) * tb;
#pragma unroll
            for (int ni = 0; ni < 2; ++ni)
#pragma unroll
                for (int gq = 0; gq < 4; ++gq) {
                    const int P = ((n0 - (isk ? DM : 0) + (ni ^ (wm & 1)) * 32) >> 2) + 2 * gq + hh;
                    const int head = P / 5, pc = P - head * 5;
                    const bool odd = r & 1;
                    const int off = base + head * 64 * tb + (pc < 4 ? (pc & 1) * 512 + (pc >> 1) * 256 : 1024) + (r & ~1) * 8 + ((r & 1) ^ wn) * tb;
                    unsigned w[2][2];
#pragma unroll
                    for (int mi = 0; mi < 2; ++mi) {
                        const f32x4 a = {acc[mi][ni][4 * gq], acc[mi][ni][4 * gq + 1], acc[mi][ni][4 * gq + 2], acc[mi][ni][4 * gq + 3]};
                        const f32x4 v = a * sc[mi] + cc[ni][gq] * cm[mi];
                        w[mi][0] = cvtpk(v[0], v[1]); w[mi][1] = cvtpk(v[2], v[3]);
                    }
                    const unsigned r0 = __builtin_amdgcn_mov_dpp(odd ? w[0][0] : w[1][0], 0xB1, 0xf, 0xf, true);
                    const unsigned r1 = __builtin_amdgcn_mov_dpp(odd ? w[0][1] : w[1][1], 0xB1, 0xf, 0xf, true);
                    __builtin_amdgcn_raw_buffer_store_b128((u32x4){odd ? r0 : w[0][0], odd ? r1 : w[0][1], odd ? w[1][0] : r0, odd ? w[1][1] : r1},
                                                           rsrc, off, 0, AUX_WT);
                }
        }
    }
}

constexpr int AT_STG = 8 * KVT_B, K2_LDS = 8 * 20 * 1024;
constexpr int GREG = 4 * 1280;
typedef __attribute__((address_space(1))) unsigned gu32;
#define BARRIER() asm volatile("s_barrier" ::: "memory")
DI void mfma16(f32x4& c, bf16x8 a, bf16x8 b, bool first) {
    if (first) asm volatile("v_mfma_f32_16x16x32_bf16 %0, %1, %2, 0" : "=&v"(c) : "v"(a), "v"(b));
    else asm volatile("v_mfma_f32_16x16x32_bf16 %0, %1, %2, %0" : "+v"(c) : "v"(a), "v"(b));
}

__global__ __launch_bounds__(768) void k2_attn(const u16* __restrict__ x, const u16* __restrict__ b2, char* __restrict__ ws,
                                               u16* __restrict__ out) {
    extern __shared__ __attribute__((aligned(16))) char smem[];
    const int tid = threadIdx.x, lane = tid & 63, wid = __builtin_amdgcn_readfirstlane(tid >> 6);
    const int hh = lane >> 5, r = lane & 31;
    const int g8 = blockIdx.x & 7, idx = blockIdx.x >> 3;
    const int head = g8 * 4 + (idx >> 3), qt = (idx & 7) * 8 + wid;
    const int tb = g8 * 4 + (idx >> 3), nb = idx & 7;
    const unsigned lds0 = (unsigned)(uintptr_t)smem;
    const lds_cptr lds = (lds_cptr)smem;
    const unsigned l16 = lane * 16;
    const auto rsrc = __builtin_amdgcn_make_buffer_rsrc(ws, 0, (int)WS_END, 0x00020000);
    f32x4* red = (f32x4*)smem;
    auto rpos = [&](int reg, int i, int u) { return (reg * 20 + i * 5 + u) * 64 + (lane ^ (2 * u + hh)); };
    auto cpos = [&](int row, int ch) { return ((row >> 4) * 5 + (ch >> 1)) * 64 + (ch & 1) * 32 + ((row & 15) ^ ch); };
    if (wid >= 8) {
        const int gw = wid - 8, k0 = gw < 2 ? gw * 14 : gw * 13 + 2;
        const int abase = (int)WS_H + (tb * 4 * 54 + k0) * 1024, wbase = (int)WS_W2F + (nb * 5 * 54 + k0) * 1024;
        const auto rs_t = __builtin_amdgcn_make_buffer_rsrc(ws, 0, gw < 2 ? (int)WS_END : 0, 0x00020000);
        bf16x8 af[2][4], wf[2][5];
        f32x4 acc[4][5];
        auto ld = [&](int j) {
#pragma unroll
            for (int u = 0; u < 5; ++u) {
                wf[j & 1][u] = __builtin_bit_cast(bf16x8, __builtin_amdgcn_raw_buffer_load_b128(j == 13 ? rs_t : rsrc, l16, wbase + (u * 54 + j) * 1024, 0));
                if (u < 4) af[j & 1][u] = __builtin_bit_cast(bf16x8, __builtin_amdgcn_raw_buffer_load_b128(j == 13 ? rs_t : rsrc, l16, abase + (u * 54 + j) * 1024, 0));
            }
            SB0();
        };
        ld(0);
        u32x2 bb[5];
#pragma unroll
        for (int u = 0; u < 5; ++u) bb[u] = *(const u32x2*)(b2 + nb * 80 + u * 16 + (lane >> 4) * 4);
#pragma unroll
        for (int j = 0; j < 14; ++j) {
            if ((j & 1) == 0) BARRIER();
            if (j == 12) { BARRIER(); BARRIER(); }
            if (j == 0) {
                f32x4 bv[5];
#pragma unroll
                for (int u = 0; u < 5; ++u) {
                    if (gw) bb[u] = (u32x2){0u, 0u};
                    bv[u] = (f32x4){bf_lo(bb[u][0]), bf_hi(bb[u][0]), bf_lo(bb[u][1]), bf_hi(bb[u][1])};
                }
                asm volatile("s_nop 3" : "+v"(bv[0]), "+v"(bv[1]), "+v"(bv[2]), "+v"(bv[3]), "+v"(bv[4]));
#pragma unroll
                for (int i = 0; i < 4; ++i)
#pragma unroll
                    for (int u = 0; u < 5; ++u)
                        asm volatile("v_mfma_f32_16x16x32_bf16 %0, %1, %2, %3" : "=&v"(acc[i][u]) : "v"(wf[0][u]), "v"(af[0][i]), "v"(bv[u]));
            } else {
#pragma unroll
                for (int i = 0; i < 4; ++i)
#pragma unroll
                    for (int u = 0; u < 5; ++u) mfma16(acc[i][u], wf[j & 1][u], af[j & 1][i], false);
            }
            SB0();
            if (j == 0) ld(1);
            if (j + 2 < 14) ld(j + 2);
        }
        asm volatile("s_nop 15\n\ts_nop 15");
        SB0();
#pragma unroll
        for (int i = 0; i < 4; ++i)
#pragma unroll
            for (int u = 0; u < 5; ++u) red[GREG + rpos(gw, i, u)] = acc[i][u];
        BAR_L();
        f32x4* src = red + GREG + gw * 64 + lane;
        f32x4 pt[5][4];
#pragma unroll
        for (int c = 0; c < 5; ++c)
#pragma unroll
            for (int w = 0; w < 4; ++w) pt[c][w] = src[c * 256 + w * 1280];
        SB0();
#pragma unroll
        for (int c = 0; c < 5; ++c) {
            f32x4 g = (pt[c][0] + pt[c][1]) + (pt[c][2] + pt[c][3]);
#pragma unroll
            for (int e = 0; e < 4; ++e) g[e] = g[e] * __builtin_amdgcn_rcpf(1.0f + __builtin_amdgcn_exp2f(-LOG2E * g[e]));
            src[c * 256 + 1280] = g;
        }
        BAR_L();
        return;
    }
    {
    __builtin_amdgcn_s_setprio(2);
    const char* kvsrc = ws + WS_KV + (size_t)(head * 64 + wid) * KVT_B;
    auto dma = [&](int st, int i) { glds16s(kvsrc + (size_t)st * AT_STG + i * 1024, l16, lds0 + (st % 3) * AT_STG + wid * KVT_B + i * 1024); };
    const char* qsrc = ws + WS_Q + (size_t)(head * 64 + qt) * QT_B;
    u32x2 q1a, q1b, q2u;
    asm volatile("global_load_dwordx2 %0, %1, %2" : "=v"(q1a) : "v"(lane * 8), "s"(qsrc) : "memory");
    asm volatile("global_load_dwordx2 %0, %1, %2 offset:512" : "=v"(q1b) : "v"(lane * 8), "s"(qsrc) : "memory");
    asm volatile("global_load_dwordx2 %0, %1, %2 offset:1024" : "=v"(q2u) : "v"(r * 8), "s"(qsrc) : "memory");
#pragma unroll
    for (int st = 0; st < 3; ++st)
#pragma unroll
        for (int i = 0; i < 3; ++i) dma(st, i);

    const int mh = wid & 1, kq = wid >> 1;
    const int abase = (int)WS_O + ((tb * 4 + mh * 2) * 20 + kq * 5) * 1024, wbase = (int)WS_WOF + (nb * 5 * 20 + kq * 5) * 1024;
    bf16x8 af[5][2], wf[4][5];
    f32x4 acc[2][5];
    u32x4 xx[2];
    auto ldw1 = [&](int j, int u) {
        wf[j & 3][u] = __builtin_bit_cast(bf16x8, __builtin_amdgcn_raw_buffer_load_b128(rsrc, l16, wbase + (u * 20 + j) * 1024, 0));
    };
    auto ldw = [&](int j) {
#pragma unroll
        for (int u = 0; u < 5; ++u) ldw1(j, u);
    };
    const int reff = r < 21 ? r : 21;
    const lds_cptr k1p = lds + lane * 8, k2p = lds + 1024 + lane * 8, vp = lds + 1536 + (hh * 24 + reff) * 16;
    auto toff = [](int T) { return ((T >> 3) % 3) * AT_STG + (T & 7) * KVT_B; };
    auto ldk1 = [&](int T) { return cat8(lds64(k1p + toff(T)), lds64(k1p + toff(T) + 512)); };
    f32x16 zero16;
#pragma unroll
    for (int e = 0; e < 16; ++e) zero16[e] = 0.f;
    f32x16 o = zero16;

    auto set_m = [&](float m) {
        const unsigned a = cvtpk(-m, 0.f) & 0xffffu;
        const float r1 = -m - bf_lo(a);
        const unsigned b = cvtpk(r1, 0.f) & 0xffffu;
        const float r2 = r1 - bf_lo(b);
        const unsigned c = cvtpk(r2, 0.f) & 0xffffu;
        if (hh) q2u = (u32x2){a | (b << 16), c};
    };

    asm volatile("s_waitcnt vmcnt(6)\n\ts_barrier" : "+v"(q1a), "+v"(q1b), "+v"(q2u) :: "memory");
    if (hh) q2u = (u32x2){0u, 0u};
    const bf16x8 q1 = __builtin_bit_cast(bf16x8, (u32x4){q1a[0], q1a[1], q1b[0], q1b[1]});
    {
        f32x16 s = MFMA32(ldk1(0), q1, zero16);
        s = MFMA32K8(lds64(k2p), __builtin_bit_cast(bf16x4, q2u), s);
        float m = s[0];
#pragma unroll
        for (int e = 1; e < 16; ++e) m = __builtin_fmaxf(m, s[e]);
        m = __builtin_fmaxf(m, xhalf(m));
        set_m(m);
    }
    {
        const bf16x4 q2 = __builtin_bit_cast(bf16x4, q2u);
        f32x16 sb[2];
        sb[0] = MFMA32(ldk1(0), q1, zero16);
        sb[0] = MFMA32K8(lds64(k2p), q2, sb[0]);
        bf16x8 k1 = ldk1(1), v0 = lds128(vp), v1;
        bf16x4 k2 = lds64(k2p + toff(1));
        u32x4 p0, p1;
        SB0();
        static_for<256>([&](auto GI) {
            constexpr int g = decltype(GI)::value, T = g >> 2, sl = g & 3, st = T >> 3;
            constexpr int c = T & 1, n = c ^ 1;
            if constexpr ((T & 7) == 6 && sl == 0 && st < 7) {
                if constexpr (st == 0) asm volatile("s_waitcnt vmcnt(3)\n\ts_barrier" ::: "memory");
                else asm volatile("s_waitcnt vmcnt(0)\n\ts_barrier" ::: "memory");
            }
            if constexpr (sl == 0) {
                if constexpr (T < 63) sb[n] = MFMA32(k1, q1, zero16);
                if constexpr (T < 62) k1 = ldk1(T + 2);
            } else if constexpr (sl == 1) {
                if constexpr (T > 0) o = MFMA32(v1, __builtin_bit_cast(bf16x8, p1), o);
                v1 = lds128(vp + toff(T) + 768);
            } else if constexpr (sl == 2) {
                if constexpr (T < 63) sb[n] = MFMA32K8(k2, q2, sb[n]);
                if constexpr (T < 62) k2 = lds64(k2p + toff(T + 2));
            } else {
                o = MFMA32(v0, __builtin_bit_cast(bf16x8, p0), o);
                if constexpr (T < 63) v0 = lds128(vp + toff(T + 1));
            }
            if constexpr ((T & 7) == 6 && sl < 3 && st >= 1 && st + 2 <= 7) dma(st + 2, sl);
            if constexpr (g >= 218 && g < 218 + 5 * 3) ldw1((g - 218) / 5, (g - 218) % 5);
            SB0();
            const float e0 = __builtin_amdgcn_exp2f(sb[c][4 * sl]), e1 = __builtin_amdgcn_exp2f(sb[c][4 * sl + 1]);
            const float e2 = __builtin_amdgcn_exp2f(sb[c][4 * sl + 2]), e3 = __builtin_amdgcn_exp2f(sb[c][4 * sl + 3]);
            if constexpr (sl < 2) { p0[2 * sl] = cvtpk(e0, e1); p0[2 * sl + 1] = cvtpk(e2, e3); }
            else { p1[2 * sl - 4] = cvtpk(e0, e1); p1[2 * sl - 3] = cvtpk(e2, e3); }
            SB0();
        });
        o = MFMA32(v1, __builtin_bit_cast(bf16x8, p1), o);
    }
    float l = o[8];
    { const float lx = xhalf(l); l = hh ? l : lx; }
    bool bad = !(l < 1.0e18f) || !(l > 0.25f);
#pragma unroll
    for (int e = 0; e < 12; ++e) bad |= !(__builtin_fabsf(o[e]) < 3.0e38f) && (e < 8 || !hh);
    if (__builtin_amdgcn_ballot_w64(bad) != 0) {
        const char* kvh = ws + WS_KV + (size_t)head * 64 * KVT_B;
        for (int pass = 0; pass < 2; ++pass) {
            if (pass == 0 && hh) q2u = (u32x2){0u, 0u};
            float m = -INFINITY;
            o = zero16;
            for (int T = 0; T < 64; ++T) {
                const char* kt = kvh + T * KVT_B;
                f32x16 s = MFMA32(cat8(*(const bf16x4*)(kt + lane * 8), *(const bf16x4*)(kt + 512 + lane * 8)), q1, zero16);
                s = MFMA32K8(*(const bf16x4*)(kt + 1024 + lane * 8), __builtin_bit_cast(bf16x4, q2u), s);
                if (pass == 0) {
#pragma unroll
                    for (int e = 0; e < 16; ++e) m = __builtin_fmaxf(m, s[e]);
                } else {
                    u32x4 p0, p1;
#pragma unroll
                    for (int e = 0; e < 4; ++e) {
                        p0[e] = cvtpk(__builtin_amdgcn_exp2f(s[2 * e]), __builtin_amdgcn_exp2f(s[2 * e + 1]));
                        p1[e] = cvtpk(__builtin_amdgcn_exp2f(s[8 + 2 * e]), __builtin_amdgcn_exp2f(s[9 + 2 * e]));
                    }
                    const char* vt = kt + 1536 + (hh * 24 + reff) * 16;
                    o = MFMA32(*(const bf16x8*)vt, __builtin_bit_cast(bf16x8, p0), o);
                    o = MFMA32(*(const bf16x8*)(vt + 768), __builtin_bit_cast(bf16x8, p1), o);
                }
            }
            if (pass == 0) { m = __builtin_fmaxf(m, xhalf(m)); set_m(m); }
        }
        l = o[8];
        { const float lx = xhalf(l); l = hh ? l : lx; }
    }
    const float il = 1.0f / l;
    auto pk = [&](int e) { return __builtin_elementwise_min(cvtpk(o[e] * il, o[e + 1] * il), EMPTY - 1u); };
    const int obase = (int)WS_O + (qt * 2 + (r >> 4)) * 20 * 1024 + (r & 15) * 16;
#pragma unroll
    for (int i = 0; i < 3; ++i) {
        if (i < 2 || hh == 0) {
            const int G = 5 * head + 2 * i + hh;
            __builtin_amdgcn_raw_buffer_store_b64((u32x2){pk(4 * i), pk(4 * i + 2)}, rsrc,
                                                  obase + (G >> 3) * 1024 + ((G >> 1) & 3) * 256 + (G & 1) * 8, 0, AUX_WT);
        }
    }
    BAR_L();
#pragma unroll
    for (int j = 3; j < 4; ++j) ldw(j);
    SB0();
    {
        const int pg = 5 * (kq * 8 + (lane & 7)) + 4;
        gu32* probe = (gu32*)(ws + WS_O + (unsigned)(((tb * 4 + mh * 2 + ((lane >> 3) & 1)) * 20 + (pg >> 3)) * 1024 + ((pg >> 1) & 3) * 256 + (pg & 1) * 8 + 15 * 16 + 4));
#pragma unroll 1
        for (unsigned spin = 0; spin < (1u << 22); ++spin) {
            const unsigned f = __hip_atomic_load(probe, __ATOMIC_RELAXED, __HIP_MEMORY_SCOPE_AGENT);
            if (__builtin_amdgcn_ballot_w64(f == EMPTY) == 0) break;
            ;
        }
    }
    __builtin_amdgcn_fence(__ATOMIC_ACQUIRE, "wavefront");
    auto lda = [&](int j, int i) {
        return __builtin_bit_cast(bf16x8, __builtin_amdgcn_raw_buffer_load_b128(rsrc, l16, abase + (i * 20 + j) * 1024, AUX_WT));
    };
    auto umax = [](unsigned m, bf16x8 v) {
        const u32x4 a = __builtin_bit_cast(u32x4, v);
        m = __builtin_elementwise_max(__builtin_elementwise_max(m, a[0]), a[1]);
        return __builtin_elementwise_max(__builtin_elementwise_max(m, a[2]), a[3]);
    };
#pragma unroll
    for (int j = 0; j < 5; ++j)
#pragma unroll
        for (int i = 0; i < 2; ++i) af[j][i] = lda(j, i);
    SB0();
    BARRIER();
    SB0();
    unsigned mx = 0;
#pragma unroll
    for (int j = 0; j < 5; ++j) {
#pragma unroll
        for (int i = 0; i < 2; ++i)
#pragma unroll
            for (int u = 0; u < 5; ++u) mfma16(acc[i][u], wf[j & 3][u], af[j][i], j == 0);
        SB0();
        mx = umax(umax(mx, af[j][0]), af[j][1]);
        SB0();
        if (j == 0) { ldw(4); SB0(); }
        if (j == 1) {
#pragma unroll
            for (int ps = 0; ps < 2; ++ps) {
                const int c = (wid < 2 ? ps * 512 : 0) + wid * 64 + lane_id(), row = c / 10, ch = c - row * 10;
                xx[ps] = *(const u32x4*)(x + (size_t)(tb * 64 + row) * DM + nb * 80 + ch * 8);
            }
            SB0();
        }
    }
    if (__builtin_amdgcn_ballot_w64(mx == EMPTY) != 0) {
        asm volatile("s_nop 15\n\ts_nop 15");
        SB0();
#pragma unroll
        for (int i = 0; i < 2; ++i)
#pragma unroll
            for (int u = 0; u < 5; ++u) acc[i][u] = (f32x4){0.f, 0.f, 0.f, 0.f};
#pragma unroll 1
        for (int j = 0; j < 5; ++j) {
            bf16x8 a0, a1;
#pragma unroll 1
            for (unsigned spin = 0;; ++spin) {
                asm volatile("" ::: "memory");
                a0 = lda(j, 0); a1 = lda(j, 1);
                if (__builtin_amdgcn_ballot_w64(umax(umax(0u, a0), a1) == EMPTY) == 0 || spin >= (1u << 16)) break;
                ;
            }
#pragma unroll
            for (int u = 0; u < 5; ++u) {
                const bf16x8 w = __builtin_bit_cast(bf16x8, __builtin_amdgcn_raw_buffer_load_b128(rsrc, l16, wbase + (u * 20 + j) * 1024, 0));
                acc[0][u] = __builtin_amdgcn_mfma_f32_16x16x32_bf16(w, a0, acc[0][u], 0, 0, 0);
                acc[1][u] = __builtin_amdgcn_mfma_f32_16x16x32_bf16(w, a1, acc[1][u], 0, 0, 0);
            }
        }
    }
    asm volatile("s_nop 15\n\ts_nop 15");
    SB0();
#pragma unroll
    for (int i = 0; i < 2; ++i)
#pragma unroll
        for (int u = 0; u < 5; ++u) red[rpos(kq, mh * 2 + i, u)] = acc[i][u];
    BAR_L();
#pragma unroll
    for (int ps = 0; ps < 2; ++ps) {
        if (ps == 0 || wid < 2) {
            const int c = ps * 512 + tid, row = c / 10, ch = c - row * 10;
            const f32x4* src = red + cpos(row, ch);
            const f32x4 s0 = (src[0] + src[1280]) + (src[2560] + src[3840]), s1 = (src[16] + src[1296]) + (src[2576] + src[3856]);
            const f32x4 g0 = src[GREG + 1280], g1 = src[GREG + 1296];
            const u32x4 xv = xx[ps];
            u32x4 ov;
            ov[0] = cvtpk(__builtin_fmaf(s0[0], g0[0], bf_lo(xv[0])), __builtin_fmaf(s0[1], g0[1], bf_hi(xv[0])));
            ov[1] = cvtpk(__builtin_fmaf(s0[2], g0[2], bf_lo(xv[1])), __builtin_fmaf(s0[3], g0[3], bf_hi(xv[1])));
            ov[2] = cvtpk(__builtin_fmaf(s1[0], g1[0], bf_lo(xv[2])), __builtin_fmaf(s1[1], g1[1], bf_hi(xv[2])));
            ov[3] = cvtpk(__builtin_fmaf(s1[2], g1[2], bf_lo(xv[3])), __builtin_fmaf(s1[3], g1[3], bf_hi(xv[3])));
            *(u32x4*)(out + (size_t)(tb * 64 + row) * DM + nb * 80 + ch * 8) = ov;
        }
    }
    }
}

extern "C" void kernel_launch(void* const* d_in, const int* in_sizes, int n_in, void* d_out, int out_size, void* d_ws, size_t ws_size,
                              hipStream_t stream) {
    const u16* x = (const u16*)d_in[0];
    const u16* wqkv = (const u16*)d_in[1];
    const u16* wo = (const u16*)d_in[2];
    const u16* w1 = (const u16*)d_in[4];
    const u16* b1 = (const u16*)d_in[5];
    const u16* w2 = (const u16*)d_in[6];
    const u16* b2 = (const u16*)d_in[7];
    char* ws = (char*)d_ws;
    (void)hipFuncSetAttribute((const void*)k1_proj, hipFuncAttributeMaxDynamicSharedMemorySize, K1_LDS);
    (void)hipFuncSetAttribute((const void*)k2_attn, hipFuncAttributeMaxDynamicSharedMemorySize, K2_LDS);
    k1_proj<<<256, 512, K1_LDS, stream>>>(x, wqkv, w1, b1, w2, wo, ws);
    k2_attn<<<256, 768, K2_LDS, stream>>>(x, b2, ws, (u16*)d_out);
}
```

```cpp
#include <hip/hip_runtime.h>
#include <hip/hip_bf16.h>
#include <stdint.h>
#include <type_traits>

typedef short bf16x8 __attribute__((ext_vector_type(8)));
typedef short bf16x4 __attribute__((ext_vector_type(4)));
typedef float f32x16 __attribute__((ext_vector_type(16)));
typedef float f32x4 __attribute__((ext_vector_type(4)));
typedef unsigned u32x4 __attribute__((ext_vector_type(4)));
typedef unsigned u32x2 __attribute__((ext_vector_type(2)));
typedef __bf16 bf2_t __attribute__((ext_vector_type(2)));
typedef short v4i16_t __attribute__((ext_vector_type(4)));
typedef unsigned short u16;
#define LDS_AS __attribute__((address_space(3)))
typedef LDS_AS const char* lds_cptr;
typedef LDS_AS char* lds_ptr;
#define DI __device__ __forceinline__

constexpr int SEQ = 2048, DM = 640, NH = 32, DH = 20, FF = 1728;
constexpr float LN_EPS = 1e-5f;
constexpr float LOG2E = 1.4426950408889634f;
constexpr float QSCALE = 0.22360679774997896f * LOG2E;

constexpr int QT_B = 1280, KVT_B = 3072;
constexpr size_t WS_Q = 0;
constexpr size_t WS_KV = WS_Q + (size_t)NH * 64 * QT_B;
constexpr size_t WS_H = WS_KV + (size_t)NH * 64 * KVT_B;
constexpr size_t WS_O = WS_H + (size_t)128 * 54 * 1024;
constexpr size_t WS_W2F = WS_O + (size_t)128 * 20 * 1024;
constexpr size_t WS_WOF = WS_W2F + (size_t)40 * 54 * 1024;
constexpr size_t WS_END = WS_WOF + (size_t)40 * 20 * 1024;
constexpr unsigned EMPTY = 0xffffffffu;
constexpr int AUX_WT = 16;

#define MFMA32(a, b, c) __builtin_amdgcn_mfma_f32_32x32x16_bf16((a), (b), (c), 0, 0, 0)
#define MFMA32K8(a, b, c) __builtin_amdgcn_mfma_f32_32x32x8bf16_1k((a), (b), (c), 0, 0, 0)

typedef float f32x2 __attribute__((ext_vector_type(2)));
DI unsigned cvtpk(float lo, float hi) { return __builtin_bit_cast(unsigned, __builtin_convertvector((f32x2){lo, hi}, bf2_t)); }
DI float bf_lo(unsigned u) { return __uint_as_float(u << 16); }
DI float bf_hi(unsigned u) { return __uint_as_float(u & 0xffff0000u); }
DI float dot2(unsigned a, unsigned b, float c) { return __builtin_amdgcn_fdot2_f32_bf16(__builtin_bit_cast(bf2_t, a), __builtin_bit_cast(bf2_t, b), c, false); }
DI bf16x8 lds128(lds_cptr p) { return *(const LDS_AS bf16x8*)p; }
DI bf16x4 lds64(lds_cptr p) { return *(const LDS_AS bf16x4*)p; }
DI bf16x4 ldstr(lds_cptr p) { return __builtin_bit_cast(bf16x4, __builtin_amdgcn_ds_read_tr16_b64_v4i16((LDS_AS v4i16_t*)p)); }
DI bf16x8 cat8(bf16x4 a, bf16x4 b) { return __builtin_shufflevector(a, b, 0, 1, 2, 3, 4, 5, 6, 7); }
DI float xhalf(float v) {
    auto r = __builtin_amdgcn_permlane32_swap(__float_as_uint(v), __float_as_uint(v), false, false);
    return (threadIdx.x & 32) ? __uint_as_float(r[0]) : __uint_as_float(r[1]);
}
#define WAITV(n) asm volatile("s_waitcnt vmcnt(" #n ")" ::: "memory")
#define WAITV_BAR(n) asm volatile("s_waitcnt vmcnt(" #n ") lgkmcnt(0)\n\ts_barrier" ::: "memory")
#define BAR_L() asm volatile("s_waitcnt lgkmcnt(0)\n\ts_barrier" ::: "memory")

constexpr int K1_AW = 3 * 8192;
constexpr int K1_BOFF = 4 * K1_AW, K1_LDS = 6 * K1_AW + 4096;

template <int N, class F> DI void static_for(F&& f) {
    if constexpr (N > 0) { static_for<N - 1>(f); f(std::integral_constant<int, N - 1>{}); }
}
DI void glds16s(const void* sbase, unsigned voff, unsigned lds_dst) {
    asm volatile("s_mov_b32 m0, %2\n\ts_nop 0\n\tglobal_load_lds_dwordx4 %0, %1" :: "v"(voff), "s"(sbase), "s"(lds_dst) : "memory");
}
DI int lane_id() {
    int l;
    asm volatile("v_mbcnt_lo_u32_b32 %0, -1, 0\n\tv_mbcnt_hi_u32_b32 %0, -1, %0" : "=v"(l));
    return l;
}
DI float vadd(float a, float b) { float d; asm("v_add_f32 %0, %1, %2" : "=v"(d) : "v"(a), "v"(b)); return d; }
DI float vfma(float a, float b, float c) { float d; asm("v_fma_f32 %0, %1, %2, %3" : "=v"(d) : "v"(a), "v"(b), "v"(c)); return d; }
#define SB0() __builtin_amdgcn_sched_barrier(0)

__global__ __launch_bounds__(512) void k1_proj(const u16* __restrict__ x, const u16* __restrict__ wqkv, const u16* __restrict__ w1,
                                               const u16* __restrict__ b1, const u16* __restrict__ w2, const u16* __restrict__ wo,
                                               char* __restrict__ ws) {
    extern __shared__ __attribute__((aligned(16))) char smem[];
    const int tid = threadIdx.x, lane = tid & 63, wid = __builtin_amdgcn_readfirstlane(tid >> 6);
    const int g8 = blockIdx.x & 7, idx = blockIdx.x >> 3;
    const int nq = g8 >> 1, pi = idx >> 2;
    const auto rsrc = __builtin_amdgcn_make_buffer_rsrc(ws, 0, (int)WS_END, 0x00020000);
    if (pi >= (nq == 3 ? 8 : 7)) {
        const int wv = (g8 * 4 + (idx & 3)) * 8 + wid;
        const unsigned ldsw = (unsigned)(uintptr_t)smem + wid * 10240;
        const int g = lane >> 4, q = (lane & 15) >> 2, p = lane & 3;
        for (int un = wv; un < 296; un += 192) {
            const int ksg = un >> 2, qr = un & 3;
            const bool is2 = ksg < 54;
            const int ks = is2 ? ksg : ksg - 54, nks = is2 ? 54 : 20;
            const u16* src = (is2 ? w2 : wo) + (size_t)ks * 32 * DM + qr * 160;
#pragma unroll
            for (int i = 0; i < 10; ++i) {
                const int o = i * 64 + lane, row = o / 20, ch = o - row * 20;
                glds16s(src, row * (DM * 2) + ch * 16, ldsw + i * 1024);
            }
            const lds_cptr tp = (lds_cptr)smem + wid * 10240 + ((is2 ? 16 * (g & 1) + 4 * (g >> 1) : 8 * g) + q) * 320 + p * 8;
            const int second = is2 ? 8 * 320 : 4 * 320;
            WAITV(0);
            const int dst = (int)(is2 ? WS_W2F : WS_WOF) + (qr * 10 * nks + ks) * 1024 + lane * 16;
#pragma unroll
            for (int u = 0; u < 10; ++u)
                __builtin_amdgcn_raw_buffer_store_b128(__builtin_bit_cast(u32x4, cat8(ldstr(tp + u * 32), ldstr(tp + u * 32 + second))),
                                                       rsrc, dst + u * nks * 1024, 0, AUX_WT);
            asm volatile("s_waitcnt lgkmcnt(0)" ::: "memory");
        }
        for (int i = wv * 64 + lane; i < 2048 * 24; i += 192 * 64) {
            const int tile = i / 24, c = i - tile * 24;
            const int off = c < 16 ? 1280 + c * 16 : 1536 + (((c - 16) >> 1) * 24 + 20 + (c & 1)) * 16;
            const unsigned v0 = c < 16 || !(c & 1) ? 0x3f803f80u : 0u, v1 = c < 16 ? 0x00003f80u : v0;
            __builtin_amdgcn_raw_buffer_store_b128((u32x4){v0, v1, v0, v1}, rsrc, (int)WS_KV + tile * KVT_B + off, 0, AUX_WT);
        }
        for (int i = wv; i < 128 * 20; i += 192) {
            __builtin_amdgcn_raw_buffer_store_b128((u32x4){EMPTY, EMPTY, EMPTY, EMPTY}, rsrc, (int)WS_O + i * 1024 + lane * 16, 0, AUX_WT);
            __builtin_amdgcn_s_sleep(12);
        }
        return;
    }
    const int mt = (g8 & 1) * 4 + (idx & 3), cb = nq * 7 + pi;
    const int m0 = mt * 256;
    const bool mlp0 = cb < 27;
    const int t0 = mlp0 ? cb : (cb == 27 ? 27 : 29), t1 = mlp0 ? cb : (cb == 27 ? 28 : 29);
    const u16* B0 = (mlp0 ? w1 : wqkv) + t0 * 64;
    const u16* B1 = wqkv + t1 * 64;
    const int ld0 = mlp0 ? FF : 3 * DM;
    const u16* A0 = x + (size_t)m0 * DM;
    unsigned avo[4], bvo[2];
#pragma unroll
    for (int i = 0; i < 4; ++i) {
        const int row = wid * 32 + i * 8 + (lane >> 3);
        avo[i] = row * (DM * 2) + (((lane & 7) ^ ((row >> 1) & 7)) << 4);
    }
    {
        const int row = wid * 8 + (lane >> 3), ch = (lane & 7) ^ (((row >> 1) & 1) << 2);
        bvo[0] = row * ld0 * 2 + ch * 16;
        bvo[1] = row * (3 * DM * 2) + ch * 16;
    }
    const unsigned lds0 = (unsigned)(uintptr_t)smem;
    const lds_cptr lds = (lds_cptr)smem;
    const unsigned adst = lds0 + (wid >> 1) * K1_AW + (wid & 1) * 4096, bdst = lds0 + K1_BOFF + wid * 1024;
    auto dma = [&](int st, int j) {
        const int slot = st % 3;
        if (j < 4) glds16s(A0 + st * 64, avo[j], adst + slot * 8192 + j * 1024);
        else if (j == 4) glds16s(B0 + (size_t)st * 64 * ld0, bvo[0], bdst + slot * 8192);
        else glds16s(B1 + (size_t)st * 64 * (3 * DM), bvo[1], bdst + K1_AW + slot * 8192);
    };
#pragma unroll
    for (int st = 0; st < 3; ++st)
#pragma unroll
        for (int j = 0; j < 6; ++j) dma(st, j);

    const int wm = wid & 3, wn = wid >> 2, hh = lane >> 5, r = lane & 31;
    const int tq = wn ? t1 : t0;
    const bool is_mlp = mlp0 && wn == 0;
    const bool is_v = !is_mlp && tq >= 20;
    u32x2 bb[2][4];
#pragma unroll
    for (int ni = 0; ni < 2; ++ni)
#pragma unroll
        for (int gq = 0; gq < 4; ++gq) bb[ni][gq] = *(const u32x2*)(b1 + (mlp0 ? t0 * 64 : 0) + (ni ^ (wm & 1)) * 32 + 8 * gq + 4 * hh);
    f32x16 acc[2][2];
#pragma unroll
    for (int i = 0; i < 2; ++i)
#pragma unroll
        for (int j = 0; j < 2; ++j)
#pragma unroll
            for (int e = 0; e < 16; ++e) acc[i][j][e] = 0.f;
    float rs0 = 0.f, rs1 = 0.f, rq0 = 0.f, rq1 = 0.f, rs2 = 0.f, rq2 = 0.f, cs = 0.f;

    auto run = [&](auto TR) {
        constexpr bool TRANS = decltype(TR)::value;
        const int par = wm >> 1;
        lds_cptr ap[4][2];
#pragma unroll
        for (int kk = 0; kk < 4; ++kk)
#pragma unroll
            for (int mi = 0; mi < 2; ++mi)
                ap[kk][mi] = lds + wm * K1_AW + ((mi ^ wn) * 32 + r) * 128 + (((2 * (kk ^ par) + hh) ^ ((r >> 1) & 7)) << 4);
        const int g = lane >> 4, q = (lane & 15) >> 2, p = lane & 3;
        lds_cptr bp[2][2];
#pragma unroll
        for (int ni = 0; ni < 2; ++ni)
#pragma unroll
            for (int k1 = 0; k1 < 2; ++k1)
                bp[ni][k1] = lds + K1_BOFF + wn * K1_AW + (k1 ^ par) * 2048 + (8 * (g >> 1) + q) * 128 +
                             (((((ni ^ wm ^ (q >> 1)) & 1) << 2) + 2 * (g & 1) + (p >> 1)) << 4) + (p & 1) * 8;
        bf16x8 af[2][2], bfr[2][2];
        auto lda = [&](int st, int kk, int mi) { return lds128(ap[kk][mi] + (st % 3) * 8192); };
        auto ldb = [&](int st, int kk, int ni) {
            const lds_cptr s = bp[ni][kk & 1] + (st % 3) * 8192 + (kk & 2) * 2048;
            return cat8(ldstr(s), ldstr(s + 512));
        };
        auto stat = [&](unsigned u) {
            const float lo = bf_lo(u), hi = bf_hi(u);
            rs0 = vadd(rs0, lo); rq0 = vfma(lo, lo, rq0);
            rs1 = vadd(rs1, hi); rq1 = vfma(hi, hi, rq1);
        };
        WAITV_BAR(20);
        af[0][0] = lda(0, 0, 0); af[0][1] = lda(0, 0, 1); bfr[0][0] = ldb(0, 0, 0); bfr[0][1] = ldb(0, 0, 1);
        constexpr int NV = 3;
        unsigned sa[4][4], sbv[2][4];
        auto clump = [&](int first) {
#pragma unroll
            for (int k = first; k < 4; ++k)
#pragma unroll
                for (int j = NV; j < 4; ++j) { rs2 = dot2(sa[k][j], 0x3f803f80u, rs2); rq2 = dot2(sa[k][j], sa[k][j], rq2); }
#pragma unroll
            for (int k = 0; k < 2; ++k)
#pragma unroll
                for (int j = 0; j < 4; ++j) cs = dot2(sbv[k][j], 0x3f803f80u, cs);
            SB0();
        };
        static_for<40>([&](auto S) {
            constexpr int s = decltype(S)::value, st = s >> 2, kk = s & 3, c = s & 1, n = c ^ 1;
            constexpr int ns = s + 1, nst = ns >> 2, nkk = ns & 3;
            if constexpr (kk == 3) clump(st == 0 ? 1 : 0);
            if constexpr (kk == 3 && st < 9) {
                if constexpr (st == 0) { WAITV_BAR(14); } else
                if constexpr (st < 8) { WAITV_BAR(6); } else { WAITV_BAR(0); }
            }
            constexpr int ds = (s + 1) / 4 + 2, dj = (s + 1) & 3;
            constexpr bool dm = s >= 3 && ds < 10;
            const u32x4 a = __builtin_bit_cast(u32x4, af[c][0]);
#pragma unroll
            for (int j = 0; j < 4; ++j) sa[(kk + 1) & 3][j] = a[j];
            if constexpr ((kk & 1) == 0) {
                const u32x4 b = __builtin_bit_cast(u32x4, bfr[c][0]);
#pragma unroll
                for (int j = 0; j < 4; ++j) sbv[kk >> 1][j] = b[j];
            }
#define K1_MM(mi, ni) acc[mi][ni] = TRANS ? MFMA32(bfr[c][ni], af[c][mi], acc[mi][ni]) : MFMA32(af[c][mi], bfr[c][ni], acc[mi][ni])
            K1_MM(0, 0);
            if constexpr (ns < 40) af[n][0] = lda(nst, nkk, 0);
            if constexpr (NV >= 1) stat(a[0]);
            SB0();
            K1_MM(0, 1);
            if constexpr (ns < 40) bfr[n][0] = ldb(nst, nkk, 0);
            if constexpr (dm) dma(ds, dj < 2 ? 2 * dj : dj + 2);
            if constexpr (NV >= 4) stat(a[3]);
            SB0();
            K1_MM(1, 0);
            if constexpr (ns < 40) bfr[n][1] = ldb(nst, nkk, 1);
            if constexpr (NV >= 2) stat(a[1]);
            SB0();
            K1_MM(1, 1);
            if constexpr (ns < 40) af[n][1] = lda(nst, nkk, 1);
            if constexpr (dm && dj < 2) dma(ds, 2 * dj + 1);
            if constexpr (NV >= 3) stat(a[2]);
            SB0();
        });
#pragma unroll
        for (int j = NV; j < 4; ++j) { rs2 = dot2(sa[0][j], 0x3f803f80u, rs2); rq2 = dot2(sa[0][j], sa[0][j], rq2); }
    };
    if (is_v) run(std::integral_constant<bool, false>{}); else run(std::integral_constant<bool, true>{});

    float* st_mu = (float*)(smem + 6 * K1_AW);
    float* st_r1 = st_mu + 256;
    float* st_r2 = st_r1 + 256;
    float* st_cs = st_r2 + 256;
    float rs = rs0 + rs1 + rs2, rq = rq0 + rq1 + rq2;
    rs += xhalf(rs); rq += xhalf(rq); cs += xhalf(cs);
    {
        const float mu = rs * (1.0f / DM);
        const float var = __builtin_fmaxf(rq * (1.0f / DM) - mu * mu, 0.f);
        const float r1 = __builtin_amdgcn_rsqf(var + LN_EPS);
        const float r12 = r1 * __builtin_amdgcn_rsqf(var * r1 * r1 + LN_EPS);
        float* pa = hh ? st_r2 + wm * 64 + wn * 32 + r : st_mu + wm * 64 + wn * 32 + r;
        float* pb = hh ? st_cs + (wm >> 1) * 128 + wn * 64 + (wm & 1) * 32 + r : st_r1 + wm * 64 + wn * 32 + r;
        *pa = hh ? r12 : mu;
        *pb = hh ? cs : r1;
    }
    BAR_L();
    if (cb == 28 && wn == 1) return;
    if (!is_mlp) __builtin_amdgcn_s_setprio(3);
    const int n0 = tq * 64;

    if (is_v) {
        f32x4 mu[2][4], sc[2][4];
#pragma unroll
        for (int mi = 0; mi < 2; ++mi)
#pragma unroll
            for (int q4 = 0; q4 < 4; ++q4) {
                mu[mi][q4] = *(const f32x4*)(st_mu + wm * 64 + (mi ^ wn) * 32 + 8 * q4 + 4 * hh);
                sc[mi][q4] = *(const f32x4*)(st_r1 + wm * 64 + (mi ^ wn) * 32 + 8 * q4 + 4 * hh);
            }
#pragma unroll
        for (int ni = 0; ni < 2; ++ni) {
            const int cl = (ni ^ (wm & 1)) * 32 + r;
            const float c = st_cs[wn * 64 + cl] + st_cs[128 + wn * 64 + cl];
            const int nn = n0 - 2 * DM + cl, head = nn / DH, d = nn - head * DH;
#pragma unroll
            for (int mi = 0; mi < 2; ++mi) {
                const int rl = wm * 64 + (mi ^ wn) * 32;
                const int dst = (int)WS_KV + (head * 64 + ((m0 + rl) >> 5)) * KVT_B + 1536 + (hh * 24 + d) * 16;
#pragma unroll
                for (int s = 0; s < 2; ++s) {
                    u32x4 o;
#pragma unroll
                    for (int gq = 0; gq < 2; ++gq) {
                        const f32x4 a = {acc[mi][ni][8 * s + 4 * gq], acc[mi][ni][8 * s + 4 * gq + 1], acc[mi][ni][8 * s + 4 * gq + 2], acc[mi][ni][8 * s + 4 * gq + 3]};
                        const f32x4 v = (a - mu[mi][2 * s + gq] * c) * sc[mi][2 * s + gq];
                        o[2 * gq] = cvtpk(v[0], v[1]); o[2 * gq + 1] = cvtpk(v[2], v[3]);
                    }
                    __builtin_amdgcn_raw_buffer_store_b128(o, rsrc, dst + s * 768, 0, AUX_WT);
                }
            }
        }
    } else {
        const bool isk = tq >= 10;
        float sc[2], cm[2];
#pragma unroll
        for (int mi = 0; mi < 2; ++mi) {
            const int rl = wm * 64 + (mi ^ wn) * 32 + r;
            sc[mi] = is_mlp ? st_r2[rl] : st_r1[rl] * (isk ? 1.0f : QSCALE);
            cm[mi] = -st_mu[rl] * sc[mi];
        }
        f32x4 cc[2][4];
#pragma unroll
        for (int ni = 0; ni < 2; ++ni)
#pragma unroll
            for (int gq = 0; gq < 4; ++gq) {
                const int cl = wn * 64 + (ni ^ (wm & 1)) * 32 + 8 * gq + 4 * hh;
                cc[ni][gq] = *(const f32x4*)(st_cs + cl) + *(const f32x4*)(st_cs + 128 + cl);
            }
        if (is_mlp) {
            float c1 = -2.0f * 0.7978845608028654f * LOG2E, c2 = c1 * 0.044715f;
            asm volatile("" : "+v"(c1), "+v"(c2));
#pragma unroll
            for (int ni = 0; ni < 2; ++ni) {
#pragma unroll
                for (int mi = 0; mi < 2; ++mi) {
                    unsigned pk[8];
#pragma unroll
                    for (int gq = 0; gq < 4; ++gq) {
                        const f32x4 bv = {bf_lo(bb[ni][gq][0]), bf_hi(bb[ni][gq][0]), bf_lo(bb[ni][gq][1]), bf_hi(bb[ni][gq][1])};
                        const f32x4 a = {acc[mi][ni][4 * gq], acc[mi][ni][4 * gq + 1], acc[mi][ni][4 * gq + 2], acc[mi][ni][4 * gq + 3]};
                        const f32x4 u = a * sc[mi] + (cc[ni][gq] * cm[mi] + bv);
                        const f32x4 t = u * (u * u * c2 + c1);
                        f32x4 d;
#pragma unroll
                        for (int e = 0; e < 4; ++e) d[e] = __builtin_amdgcn_exp2f(t[e]);
                        d = d + 1.0f;
#pragma unroll
                        for (int e = 0; e < 4; ++e) d[e] = __builtin_amdgcn_rcpf(d[e]);
                        const f32x4 v = u * d;
                        pk[2 * gq] = cvtpk(v[0], v[1]); pk[2 * gq + 1] = cvtpk(v[2], v[3]);
                    }
                    const int t16 = (m0 + wm * 64 + mi * 32 + r) >> 4;
                    const int dst = (int)WS_H + (t16 * 54 + ((n0 + (ni ^ (wm & 1)) * 32) >> 5)) * 1024 + (hh * 32 + (r & 15)) * 16;
                    __builtin_amdgcn_raw_buffer_store_b128((u32x4){pk[0], pk[1], pk[2], pk[3]}, rsrc, dst, 0, AUX_WT);
                    __builtin_amdgcn_raw_buffer_store_b128((u32x4){pk[4], pk[5], pk[6], pk[7]}, rsrc, dst + 256, 0, AUX_WT);
                }
            }
        } else {
            const int tb = isk ? KVT_B : QT_B;
            const int base = (int)(isk ? WS_KV : WS_Q) + ((m0 + wm * 64) >> 5) * tb;
#pragma unroll
            for (int ni = 0; ni < 2; ++ni)
#pragma unroll
                for (int gq = 0; gq < 4; ++gq) {
                    const int P = ((n0 - (isk ? DM : 0) + (ni ^ (wm & 1)) * 32) >> 2) + 2 * gq + hh;
                    const int head = P / 5, pc = P - head * 5;
                    const bool odd = r & 1;
                    const int off = base + head * 64 * tb + (pc < 4 ? (pc & 1) * 512 + (pc >> 1) * 256 : 1024) + (r & ~1) * 8 + ((r & 1) ^ wn) * tb;
                    unsigned w[2][2];
#pragma unroll
                    for (int mi = 0; mi < 2; ++mi) {
                        const f32x4 a = {acc[mi][ni][4 * gq], acc[mi][ni][4 * gq + 1], acc[mi][ni][4 * gq + 2], acc[mi][ni][4 * gq + 3]};
                        const f32x4 v = a * sc[mi] + cc[ni][gq] * cm[mi];
                        w[mi][0] = cvtpk(v[0], v[1]); w[mi][1] = cvtpk(v[2], v[3]);
                    }
                    const unsigned r0 = __builtin_amdgcn_mov_dpp(odd ? w[0][0] : w[1][0], 0xB1, 0xf, 0xf, true);
                    const unsigned r1 = __builtin_amdgcn_mov_dpp(odd ? w[0][1] : w[1][1], 0xB1, 0xf, 0xf, true);
                    __builtin_amdgcn_raw_buffer_store_b128((u32x4){odd ? r0 : w[0][0], odd ? r1 : w[0][1], odd ? w[1][0] : r0, odd ? w[1][1] : r1},
                                                           rsrc, off, 0, AUX_WT);
                }
        }
    }
}

constexpr int AT_STG = 8 * KVT_B, K2_LDS = 8 * 20 * 1024;
constexpr int GREG = 4 * 1280;
typedef __attribute__((address_space(1))) unsigned gu32;
#define BARRIER() asm volatile("s_barrier" ::: "memory")
DI void mfma16(f32x4& c, bf16x8 a, bf16x8 b, bool first) {
    if (first) asm volatile("v_mfma_f32_16x16x32_bf16 %0, %1, %2, 0" : "=&v"(c) : "v"(a), "v"(b));
    else asm volatile("v_mfma_f32_16x16x32_bf16 %0, %1, %2, %0" : "+v"(c) : "v"(a), "v"(b));
}

__global__ __launch_bounds__(768) void k2_attn(const u16* __restrict__ x, const u16* __restrict__ b2, char* __restrict__ ws,
                                               u16* __restrict__ out) {
    extern __shared__ __attribute__((aligned(16))) char smem[];
    const int tid = threadIdx.x, lane = tid & 63, wid = __builtin_amdgcn_readfirstlane(tid >> 6);
    const int hh = lane >> 5, r = lane & 31;
    const int g8 = blockIdx.x & 7, idx = blockIdx.x >> 3;
    const int head = g8 * 4 + (idx >> 3), qt = (idx & 7) * 8 + wid;
    const int tb = g8 * 4 + (idx >> 3), nb = idx & 7;
    const unsigned lds0 = (unsigned)(uintptr_t)smem;
    const lds_cptr lds = (lds_cptr)smem;
    const unsigned l16 = lane * 16;
    const auto rsrc = __builtin_amdgcn_make_buffer_rsrc(ws, 0, (int)WS_END, 0x00020000);
    f32x4* red = (f32x4*)smem;
    auto rpos = [&](int reg, int i, int u) { return (reg * 20 + i * 5 + u) * 64 + (lane ^ (2 * u + hh)); };
    auto cpos = [&](int row, int ch) { return ((row >> 4) * 5 + (ch >> 1)) * 64 + (ch & 1) * 32 + ((row & 15) ^ ch); };
    if (wid >= 8) {
        const int gw = wid - 8, k0 = gw < 2 ? gw * 14 : gw * 13 + 2;
        const int abase = (int)WS_H + (tb * 4 * 54 + k0) * 1024, wbase = (int)WS_W2F + (nb * 5 * 54 + k0) * 1024;
        const auto rs_t = __builtin_amdgcn_make_buffer_rsrc(ws, 0, gw < 2 ? (int)WS_END : 0, 0x00020000);
        bf16x8 af[2][4], wf[2][5];
        f32x4 acc[4][5];
        auto ld = [&](int j) {
#pragma unroll
            for (int u = 0; u < 5; ++u) {
                wf[j & 1][u] = __builtin_bit_cast(bf16x8, __builtin_amdgcn_raw_buffer_load_b128(j == 13 ? rs_t : rsrc, l16, wbase + (u * 54 + j) * 1024, 0));
                if (u < 4) af[j & 1][u] = __builtin_bit_cast(bf16x8, __builtin_amdgcn_raw_buffer_load_b128(j == 13 ? rs_t : rsrc, l16, abase + (u * 54 + j) * 1024, 0));
            }
            SB0();
        };
        ld(0);
        u32x2 bb[5];
#pragma unroll
        for (int u = 0; u < 5; ++u) bb[u] = *(const u32x2*)(b2 + nb * 80 + u * 16 + (lane >> 4) * 4);
#pragma unroll
        for (int j = 0; j < 14; ++j) {
            if ((j & 1) == 0) BARRIER();
            if (j == 12) { BARRIER(); BARRIER(); }
            if (j == 0) {
                f32x4 bv[5];
#pragma unroll
                for (int u = 0; u < 5; ++u) {
                    if (gw) bb[u] = (u32x2){0u, 0u};
                    bv[u] = (f32x4){bf_lo(bb[u][0]), bf_hi(bb[u][0]), bf_lo(bb[u][1]), bf_hi(bb[u][1])};
                }
                asm volatile("s_nop 3" : "+v"(bv[0]), "+v"(bv[1]), "+v"(bv[2]), "+v"(bv[3]), "+v"(bv[4]));
#pragma unroll
                for (int i = 0; i < 4; ++i)
#pragma unroll
                    for (int u = 0; u < 5; ++u)
                        asm volatile("v_mfma_f32_16x16x32_bf16 %0, %1, %2, %3" : "=&v"(acc[i][u]) : "v"(wf[0][u]), "v"(af[0][i]), "v"(bv[u]));
            } else {
#pragma unroll
                for (int i = 0; i < 4; ++i)
#pragma unroll
                    for (int u = 0; u < 5; ++u) mfma16(acc[i][u], wf[j & 1][u], af[j & 1][i], false);
            }
            SB0();
            if (j == 0) ld(1);
            if (j + 2 < 14) ld(j + 2);
        }
        asm volatile("s_nop 15\n\ts_nop 15");
        SB0();
#pragma unroll
        for (int i = 0; i < 4; ++i)
#pragma unroll
            for (int u = 0; u < 5; ++u) red[GREG + rpos(gw, i, u)] = acc[i][u];
        BAR_L();
        f32x4* src = red + GREG + gw * 64 + lane;
        f32x4 pt[5][4];
#pragma unroll
        for (int c = 0; c < 5; ++c)
#pragma unroll
            for (int w = 0; w < 4; ++w) pt[c][w] = src[c * 256 + w * 1280];
        SB0();
#pragma unroll
        for (int c = 0; c < 5; ++c) {
            f32x4 g = (pt[c][0] + pt[c][1]) + (pt[c][2] + pt[c][3]);
#pragma unroll
            for (int e = 0; e < 4; ++e) g[e] = g[e] * __builtin_amdgcn_rcpf(1.0f + __builtin_amdgcn_exp2f(-LOG2E * g[e]));
            src[c * 256 + 1280] = g;
        }
        BAR_L();
        return;
    }
    {
    __builtin_amdgcn_s_setprio(2);
    const char* kvsrc = ws + WS_KV + (size_t)(head * 64 + wid) * KVT_B;
    auto dma = [&](int st, int i) { glds16s(kvsrc + (size_t)st * AT_STG + i * 1024, l16, lds0 + (st % 3) * AT_STG + wid * KVT_B + i * 1024); };
    const char* qsrc = ws + WS_Q + (size_t)(head * 64 + qt) * QT_B;
    u32x2 q1a, q1b, q2u;
    asm volatile("global_load_dwordx2 %0, %1, %2" : "=v"(q1a) : "v"(lane * 8), "s"(qsrc) : "memory");
    asm volatile("global_load_dwordx2 %0, %1, %2 offset:512" : "=v"(q1b) : "v"(lane * 8), "s"(qsrc) : "memory");
    asm volatile("global_load_dwordx2 %0, %1, %2 offset:1024" : "=v"(q2u) : "v"(r * 8), "s"(qsrc) : "memory");
#pragma unroll
    for (int st = 0; st < 3; ++st)
#pragma unroll
        for (int i = 0; i < 3; ++i) dma(st, i);

    const int mh = wid & 1, kq = wid >> 1;
    const int abase = (int)WS_O + ((tb * 4 + mh * 2) * 20 + kq * 5) * 1024, wbase = (int)WS_WOF + (nb * 5 * 20 + kq * 5) * 1024;
    bf16x8 af[5][2], wf[4][5];
    f32x4 acc[2][5];
    u32x4 xx[2];
    auto ldw1 = [&](int j, int u) {
        wf[j & 3][u] = __builtin_bit_cast(bf16x8, __builtin_amdgcn_raw_buffer_load_b128(rsrc, l16, wbase + (u * 20 + j) * 1024, 0));
    };
    auto ldw = [&](int j) {
#pragma unroll
        for (int u = 0; u < 5; ++u) ldw1(j, u);
    };
    const int reff = r < 21 ? r : 21;
    const lds_cptr k1p = lds + lane * 8, k2p = lds + 1024 + lane * 8, vp = lds + 1536 + (hh * 24 + reff) * 16;
    auto toff = [](int T) { return ((T >> 3) % 3) * AT_STG + (T & 7) * KVT_B; };
    auto ldk1 = [&](int T) { return cat8(lds64(k1p + toff(T)), lds64(k1p + toff(T) + 512)); };
    f32x16 zero16;
#pragma unroll
    for (int e = 0; e < 16; ++e) zero16[e] = 0.f;
    f32x16 o = zero16;

    auto set_m = [&](float m) {
        const unsigned a = cvtpk(-m, 0.f) & 0xffffu;
        const float r1 = -m - bf_lo(a);
        const unsigned b = cvtpk(r1, 0.f) & 0xffffu;
        const float r2 = r1 - bf_lo(b);
        const unsigned c = cvtpk(r2, 0.f) & 0xffffu;
        if (hh) q2u = (u32x2){a | (b << 16), c};
    };

    asm volatile("s_waitcnt vmcnt(6)\n\ts_barrier" : "+v"(q1a), "+v"(q1b), "+v"(q2u) :: "memory");
    if (hh) q2u = (u32x2){0u, 0u};
    const bf16x8 q1 = __builtin_bit_cast(bf16x8, (u32x4){q1a[0], q1a[1], q1b[0], q1b[1]});
    {
        f32x16 s = MFMA32(ldk1(0), q1, zero16);
        s = MFMA32K8(lds64(k2p), __builtin_bit_cast(bf16x4, q2u), s);
        float m = s[0];
#pragma unroll
        for (int e = 1; e < 16; ++e) m = __builtin_fmaxf(m, s[e]);
        m = __builtin_fmaxf(m, xhalf(m));
        set_m(m);
    }
    {
        const bf16x4 q2 = __builtin_bit_cast(bf16x4, q2u);
        f32x16 sb[2];
        sb[0] = MFMA32(ldk1(0), q1, zero16);
        sb[0] = MFMA32K8(lds64(k2p), q2, sb[0]);
        bf16x8 k1 = ldk1(1), v0 = lds128(vp), v1;
        bf16x4 k2 = lds64(k2p + toff(1));
        u32x4 p0, p1;
        SB0();
        static_for<256>([&](auto GI) {
            constexpr int g = decltype(GI)::value, T = g >> 2, sl = g & 3, st = T >> 3;
            constexpr int c = T & 1, n = c ^ 1;
            if constexpr ((T & 7) == 6 && sl == 0 && st < 7) {
                if constexpr (st == 0) asm volatile("s_waitcnt vmcnt(3)\n\ts_barrier" ::: "memory");
                else asm volatile("s_waitcnt vmcnt(0)\n\ts_barrier" ::: "memory");
            }
            if constexpr (sl == 0) {
                if constexpr (T < 63) sb[n] = MFMA32(k1, q1, zero16);
                if constexpr (T < 62) k1 = ldk1(T + 2);
            } else if constexpr (sl == 1) {
                if constexpr (T > 0) o = MFMA32(v1, __builtin_bit_cast(bf16x8, p1), o);
                v1 = lds128(vp + toff(T) + 768);
            } else if constexpr (sl == 2) {
                if constexpr (T < 63) sb[n] = MFMA32K8(k2, q2, sb[n]);
                if constexpr (T < 62) k2 = lds64(k2p + toff(T + 2));
            } else {
                o = MFMA32(v0, __builtin_bit_cast(bf16x8, p0), o);
                if constexpr (T < 63) v0 = lds128(vp + toff(T + 1));
            }
            if constexpr ((T & 7) == 6 && sl < 3 && st >= 1 && st + 2 <= 7) dma(st + 2, sl);
            if constexpr (g >= 218 && g < 218 + 5 * 3) ldw1((g - 218) / 5, (g - 218) % 5);
            SB0();
            const float e0 = __builtin_amdgcn_exp2f(sb[c][4 * sl]), e1 = __builtin_amdgcn_exp2f(sb[c][4 * sl + 1]);
            const float e2 = __builtin_amdgcn_exp2f(sb[c][4 * sl + 2]), e3 = __builtin_amdgcn_exp2f(sb[c][4 * sl + 3]);
            if constexpr (sl < 2) { p0[2 * sl] = cvtpk(e0, e1); p0[2 * sl + 1] = cvtpk(e2, e3); }
            else { p1[2 * sl - 4] = cvtpk(e0, e1); p1[2 * sl - 3] = cvtpk(e2, e3); }
            SB0();
        });
        o = MFMA32(v1, __builtin_bit_cast(bf16x8, p1), o);
    }
    float l = o[8];
    { const float lx = xhalf(l); l = hh ? l : lx; }
    bool bad = !(l < 1.0e18f) || !(l > 0.25f);
#pragma unroll
    for (int e = 0; e < 12; ++e) bad |= !(__builtin_fabsf(o[e]) < 3.0e38f) && (e < 8 || !hh);
    if (__builtin_amdgcn_ballot_w64(bad) != 0) {
        const char* kvh = ws + WS_KV + (size_t)head * 64 * KVT_B;
        for (int pass = 0; pass < 2; ++pass) {
            if (pass == 0 && hh) q2u = (u32x2){0u, 0u};
            float m = -INFINITY;
            o = zero16;
            for (int T = 0; T < 64; ++T) {
                const char* kt = kvh + T * KVT_B;
                f32x16 s = MFMA32(cat8(*(const bf16x4*)(kt + lane * 8), *(const bf16x4*)(kt + 512 + lane * 8)), q1, zero16);
                s = MFMA32K8(*(const bf16x4*)(kt + 1024 + lane * 8), __builtin_bit_cast(bf16x4, q2u), s);
                if (pass == 0) {
#pragma unroll
                    for (int e = 0; e < 16; ++e) m = __builtin_fmaxf(m, s[e]);
                } else {
                    u32x4 p0, p1;
#pragma unroll
                    for (int e = 0; e < 4; ++e) {
                        p0[e] = cvtpk(__builtin_amdgcn_exp2f(s[2 * e]), __builtin_amdgcn_exp2f(s[2 * e + 1]));
                        p1[e] = cvtpk(__builtin_amdgcn_exp2f(s[8 + 2 * e]), __builtin_amdgcn_exp2f(s[9 + 2 * e]));
                    }
                    const char* vt = kt + 1536 + (hh * 24 + reff) * 16;
                    o = MFMA32(*(const bf16x8*)vt, __builtin_bit_cast(bf16x8, p0), o);
                    o = MFMA32(*(const bf16x8*)(vt + 768), __builtin_bit_cast(bf16x8, p1), o);
                }
            }
            if (pass == 0) { m = __builtin_fmaxf(m, xhalf(m)); set_m(m); }
        }
        l = o[8];
        { const float lx = xhalf(l); l = hh ? l : lx; }
    }
    const float il = 1.0f / l;
    auto pk = [&](int e) { return __builtin_elementwise_min(cvtpk(o[e] * il, o[e + 1] * il), EMPTY - 1u); };
    const int obase = (int)WS_O + (qt * 2 + (r >> 4)) * 20 * 1024 + (r & 15) * 16;
#pragma unroll
    for (int i = 0; i < 3; ++i) {
        if (i < 2 || hh == 0) {
            const int G = 5 * head + 2 * i + hh;
            __builtin_amdgcn_raw_buffer_store_b64((u32x2){pk(4 * i), pk(4 * i + 2)}, rsrc,
                                                  obase + (G >> 3) * 1024 + ((G >> 1) & 3) * 256 + (G & 1) * 8, 0, AUX_WT);
        }
    }
    BAR_L();
#pragma unroll
    for (int j = 3; j < 4; ++j) ldw(j);
    SB0();
    unsigned pf[2], cnt;
    {
        const int pg = 5 * (kq * 8 + (lane & 7)) + 4;
        const unsigned po = ((tb * 4 + mh * 2 + ((lane >> 3) & 1)) * 20 + (pg >> 3)) * 1024 + ((pg >> 1) & 3) * 256 + (pg & 1) * 8 + 15 * 16 + 4;
        asm volatile("global_load_dword %0, %3, %4 sc1\n\t"
                     "s_sleep 10\n\tglobal_load_dword %1, %3, %4 sc1\n\t"
                     "s_mov_b32 %2, 0x200000\n1:\n\t"
                     "s_waitcnt vmcnt(1)\n\tv_cmp_eq_u32 vcc, -1, %0\n\ts_cbranch_vccz 2f\n\tglobal_load_dword %0, %3, %4 sc1\n\t"
                     "s_waitcnt vmcnt(1)\n\tv_cmp_eq_u32 vcc, -1, %1\n\ts_cbranch_vccz 2f\n\tglobal_load_dword %1, %3, %4 sc1\n\t"
                     "s_sub_u32 %2, %2, 1\n\ts_cmp_lg_u32 %2, 0\n\ts_cbranch_scc1 1b\n2:"
                     : "=&v"(pf[0]), "=&v"(pf[1]), "=&s"(cnt) : "v"(po), "s"(ws + WS_O) : "vcc", "scc", "memory");
    }
    __builtin_amdgcn_fence(__ATOMIC_ACQUIRE, "wavefront");
    auto lda = [&](int j, int i) {
        return __builtin_bit_cast(bf16x8, __builtin_amdgcn_raw_buffer_load_b128(rsrc, l16, abase + (i * 20 + j) * 1024, AUX_WT));
    };
    auto umax = [](unsigned m, bf16x8 v) {
        const u32x4 a = __builtin_bit_cast(u32x4, v);
        m = __builtin_elementwise_max(__builtin_elementwise_max(m, a[0]), a[1]);
        return __builtin_elementwise_max(__builtin_elementwise_max(m, a[2]), a[3]);
    };
#pragma unroll
    for (int j = 0; j < 5; ++j)
#pragma unroll
        for (int i = 0; i < 2; ++i) af[j][i] = lda(j, i);
    SB0();
    asm volatile("s_waitcnt vmcnt(10)" :: "v"(pf[0]), "v"(pf[1]));
    SB0();
    BARRIER();
    SB0();
    unsigned mx = 0;
#pragma unroll
    for (int j = 0; j < 5; ++j) {
#pragma unroll
        for (int i = 0; i < 2; ++i)
#pragma unroll
            for (int u = 0; u < 5; ++u) mfma16(acc[i][u], wf[j & 3][u], af[j][i], j == 0);
        SB0();
        mx = umax(umax(mx, af[j][0]), af[j][1]);
        SB0();
        if (j == 0) { ldw(4); SB0(); }
        if (j == 1) {
#pragma unroll
            for (int ps = 0; ps < 2; ++ps) {
                const int c = (wid < 2 ? ps * 512 : 0) + wid * 64 + lane_id(), row = c / 10, ch = c - row * 10;
                xx[ps] = *(const u32x4*)(x + (size_t)(tb * 64 + row) * DM + nb * 80 + ch * 8);
            }
            SB0();
        }
    }
    if (__builtin_amdgcn_ballot_w64(mx == EMPTY) != 0) {
        asm volatile("s_nop 15\n\ts_nop 15");
        SB0();
#pragma unroll
        for (int i = 0; i < 2; ++i)
#pragma unroll
            for (int u = 0; u < 5; ++u) acc[i][u] = (f32x4){0.f, 0.f, 0.f, 0.f};
#pragma unroll 1
        for (int j = 0; j < 5; ++j) {
            bf16x8 a0, a1;
#pragma unroll 1
            for (unsigned spin = 0;; ++spin) {
                asm volatile("" ::: "memory");
                a0 = lda(j, 0); a1 = lda(j, 1);
                if (__builtin_amdgcn_ballot_w64(umax(umax(0u, a0), a1) == EMPTY) == 0 || spin >= (1u << 16)) break;
                __builtin_amdgcn_s_sleep(2);
            }
#pragma unroll
            for (int u = 0; u < 5; ++u) {
                const bf16x8 w = __builtin_bit_cast(bf16x8, __builtin_amdgcn_raw_buffer_load_b128(rsrc, l16, wbase + (u * 20 + j) * 1024, 0));
                acc[0][u] = __builtin_amdgcn_mfma_f32_16x16x32_bf16(w, a0, acc[0][u], 0, 0, 0);
                acc[1][u] = __builtin_amdgcn_mfma_f32_16x16x32_bf16(w, a1, acc[1][u], 0, 0, 0);
            }
        }
    }
    asm volatile("s_nop 15\n\ts_nop 15");
    SB0();
#pragma unroll
    for (int i = 0; i < 2; ++i)
#pragma unroll
        for (int u = 0; u < 5; ++u) red[rpos(kq, mh * 2 + i, u)] = acc[i][u];
    BAR_L();
#pragma unroll
    for (int ps = 0; ps < 2; ++ps) {
        if (ps == 0 || wid < 2) {
            const int c = ps * 512 + tid, row = c / 10, ch = c - row * 10;
            const f32x4* src = red + cpos(row, ch);
            const f32x4 s0 = (src[0] + src[1280]) + (src[2560] + src[3840]), s1 = (src[16] + src[1296]) + (src[2576] + src[3856]);
            const f32x4 g0 = src[GREG + 1280], g1 = src[GREG + 1296];
            const u32x4 xv = xx[ps];
            u32x4 ov;
            ov[0] = cvtpk(__builtin_fmaf(s0[0], g0[0], bf_lo(xv[0])), __builtin_fmaf(s0[1], g0[1], bf_hi(xv[0])));
            ov[1] = cvtpk(__builtin_fmaf(s0[2], g0[2], bf_lo(xv[1])), __builtin_fmaf(s0[3], g0[3], bf_hi(xv[1])));
            ov[2] = cvtpk(__builtin_fmaf(s1[0], g1[0], bf_lo(xv[2])), __builtin_fmaf(s1[1], g1[1], bf_hi(xv[2])));
            ov[3] = cvtpk(__builtin_fmaf(s1[2], g1[2], bf_lo(xv[3])), __builtin_fmaf(s1[3], g1[3], bf_hi(xv[3])));
            *(u32x4*)(out + (size_t)(tb * 64 + row) * DM + nb * 80 + ch * 8) = ov;
        }
    }
    }
}

extern "C" void kernel_launch(void* const* d_in, const int* in_sizes, int n_in, void* d_out, int out_size, void* d_ws, size_t ws_size,
                              hipStream_t stream) {
    const u16* x = (const u16*)d_in[0];
    const u16* wqkv = (const u16*)d_in[1];
    const u16* wo = (const u16*)d_in[2];
    const u16* w1 = (const u16*)d_in[4];
    const u16* b1 = (const u16*)d_in[5];
    const u16* w2 = (const u16*)d_in[6];
    const u16* b2 = (const u16*)d_in[7];
    char* ws = (char*)d_ws;
    (void)hipFuncSetAttribute((const void*)k1_proj, hipFuncAttributeMaxDynamicSharedMemorySize, K1_LDS);
    (void)hipFuncSetAttribute((const void*)k2_attn, hipFuncAttributeMaxDynamicSharedMemorySize, K2_LDS);
    k1_proj<<<256, 512, K1_LDS, stream>>>(x, wqkv, w1, b1, w2, wo, ws);
    k2_attn<<<256, 768, K2_LDS, stream>>>(x, b2, ws, (u16*)d_out);
}
```

```cpp
#include <hip/hip_runtime.h>
#include <hip/hip_bf16.h>
#include <stdint.h>
#include <type_traits>

typedef short bf16x8 __attribute__((ext_vector_type(8)));
typedef short bf16x4 __attribute__((ext_vector_type(4)));
typedef float f32x16 __attribute__((ext_vector_type(16)));
typedef float f32x4 __attribute__((ext_vector_type(4)));
typedef unsigned u32x4 __attribute__((ext_vector_type(4)));
typedef unsigned u32x2 __attribute__((ext_vector_type(2)));
typedef __bf16 bf2_t __attribute__((ext_vector_type(2)));
typedef short v4i16_t __attribute__((ext_vector_type(4)));
typedef unsigned short u16;
#define LDS_AS __attribute__((address_space(3)))
typedef LDS_AS const char* lds_cptr;
typedef LDS_AS char* lds_ptr;
#define DI __device__ __forceinline__

constexpr int SEQ = 2048, DM = 640, NH = 32, DH = 20, FF = 1728;
constexpr float LN_EPS = 1e-5f;
constexpr float LOG2E = 1.4426950408889634f;
constexpr float QSCALE = 0.22360679774997896f * LOG2E;

constexpr int QT_B = 1280, KVT_B = 3072;
constexpr size_t WS_Q = 0;
constexpr size_t WS_KV = WS_Q + (size_t)NH * 64 * QT_B;
constexpr size_t WS_H = WS_KV + (size_t)NH * 64 * KVT_B;
constexpr size_t WS_O = WS_H + (size_t)128 * 54 * 1024;
constexpr size_t WS_W2F = WS_O + (size_t)128 * 20 * 1024;
constexpr size_t WS_WOF = WS_W2F + (size_t)40 * 54 * 1024;
constexpr size_t WS_END = WS_WOF + (size_t)40 * 20 * 1024;
constexpr unsigned EMPTY = 0xffffffffu;
constexpr int AUX_WT = 16;

#define MFMA32(a, b, c) __builtin_amdgcn_mfma_f32_32x32x16_bf16((a), (b), (c), 0, 0, 0)
#define MFMA32K8(a, b, c) __builtin_amdgcn_mfma_f32_32x32x8bf16_1k((a), (b), (c), 0, 0, 0)

typedef float f32x2 __attribute__((ext_vector_type(2)));
DI unsigned cvtpk(float lo, float hi) { return __builtin_bit_cast(unsigned, __builtin_convertvector((f32x2){lo, hi}, bf2_t)); }
DI float bf_lo(unsigned u) { return __uint_as_float(u << 16); }
DI float bf_hi(unsigned u) { return __uint_as_float(u & 0xffff0000u); }
DI float dot2(unsigned a, unsigned b, float c) { return __builtin_amdgcn_fdot2_f32_bf16(__builtin_bit_cast(bf2_t, a), __builtin_bit_cast(bf2_t, b), c, false); }
DI bf16x8 lds128(lds_cptr p) { return *(const LDS_AS bf16x8*)p; }
DI bf16x4 lds64(lds_cptr p) { return *(const LDS_AS bf16x4*)p; }
DI bf16x4 ldstr(lds_cptr p) { return __builtin_bit_cast(bf16x4, __builtin_amdgcn_ds_read_tr16_b64_v4i16((LDS_AS v4i16_t*)p)); }
DI bf16x8 cat8(bf16x4 a, bf16x4 b) { return __builtin_shufflevector(a, b, 0, 1, 2, 3, 4, 5, 6, 7); }
DI float xhalf(float v) {
    auto r = __builtin_amdgcn_permlane32_swap(__float_as_uint(v), __float_as_uint(v), false, false);
    return (threadIdx.x & 32) ? __uint_as_float(r[0]) : __uint_as_float(r[1]);
}
#define WAITV(n) asm volatile("s_waitcnt vmcnt(" #n ")" ::: "memory")
#define WAITV_BAR(n) asm volatile("s_waitcnt vmcnt(" #n ") lgkmcnt(0)\n\ts_barrier" ::: "memory")
#define BAR_L() asm volatile("s_waitcnt lgkmcnt(0)\n\ts_barrier" ::: "memory")

constexpr int K1_AW = 3 * 8192;
constexpr int K1_BOFF = 4 * K1_AW, K1_LDS = 6 * K1_AW + 4096;

template <int N, class F> DI void static_for(F&& f) {
    if constexpr (N > 0) { static_for<N - 1>(f); f(std::integral_constant<int, N - 1>{}); }
}
DI void glds16s(const void* sbase, unsigned voff, unsigned lds_dst) {
    asm volatile("s_mov_b32 m0, %2\n\ts_nop 0\n\tglobal_load_lds_dwordx4 %0, %1" :: "v"(voff), "s"(sbase), "s"(lds_dst) : "memory");
}
DI int lane_id() {
    int l;
    asm volatile("v_mbcnt_lo_u32_b32 %0, -1, 0\n\tv_mbcnt_hi_u32_b32 %0, -1, %0" : "=v"(l));
    return l;
}
DI float vadd(float a, float b) { float d; asm("v_add_f32 %0, %1, %2" : "=v"(d) : "v"(a), "v"(b)); return d; }
DI float vfma(float a, float b, float c) { float d; asm("v_fma_f32 %0, %1, %2, %3" : "=v"(d) : "v"(a), "v"(b), "v"(c)); return d; }
#define SB0() __builtin_amdgcn_sched_barrier(0)

__global__ __launch_bounds__(512) void k1_proj(const u16* __restrict__ x, const u16* __restrict__ wqkv, const u16* __restrict__ w1,
                                               const u16* __restrict__ b1, const u16* __restrict__ w2, const u16* __restrict__ wo,
                                               char* __restrict__ ws) {
    extern __shared__ __attribute__((aligned(16))) char smem[];
    const int tid = threadIdx.x, lane = tid & 63, wid = __builtin_amdgcn_readfirstlane(tid >> 6);
    const int g8 = blockIdx.x & 7, idx = blockIdx.x >> 3;
    const int nq = g8 >> 1, pi = idx >> 2;
    const auto rsrc = __builtin_amdgcn_make_buffer_rsrc(ws, 0, (int)WS_END, 0x00020000);
    if (pi >= (nq == 3 ? 8 : 7)) {
        const int wv = (g8 * 4 + (idx & 3)) * 8 + wid;
        const unsigned ldsw = (unsigned)(uintptr_t)smem + wid * 10240;
        const int g = lane >> 4, q = (lane & 15) >> 2, p = lane & 3;
        for (int un = wv; un < 296; un += 192) {
            const int ksg = un >> 2, qr = un & 3;
            const bool is2 = ksg < 54;
            const int ks = is2 ? ksg : ksg - 54, nks = is2 ? 54 : 20;
            const u16* src = (is2 ? w2 : wo) + (size_t)ks * 32 * DM + qr * 160;
#pragma unroll
            for (int i = 0; i < 10; ++i) {
                const int o = i * 64 + lane, row = o / 20, ch = o - row * 20;
                glds16s(src, row * (DM * 2) + ch * 16, ldsw + i * 1024);
            }
            const lds_cptr tp = (lds_cptr)smem + wid * 10240 + ((is2 ? 16 * (g & 1) + 4 * (g >> 1) : 8 * g) + q) * 320 + p * 8;
            const int second = is2 ? 8 * 320 : 4 * 320;
            WAITV(0);
            const int dst = (int)(is2 ? WS_W2F : WS_WOF) + (qr * 10 * nks + ks) * 1024 + lane * 16;
#pragma unroll
            for (int u = 0; u < 10; ++u)
                __builtin_amdgcn_raw_buffer_store_b128(__builtin_bit_cast(u32x4, cat8(ldstr(tp + u * 32), ldstr(tp + u * 32 + second))),
                                                       rsrc, dst + u * nks * 1024, 0, AUX_WT);
            asm volatile("s_waitcnt lgkmcnt(0)" ::: "memory");
        }
        for (int i = wv * 64 + lane; i < 2048 * 24; i += 192 * 64) {
            const int tile = i / 24, c = i - tile * 24;
            const int off = c < 16 ? 1280 + c * 16 : 1536 + (((c - 16) >> 1) * 24 + 20 + (c & 1)) * 16;
            const unsigned v0 = c < 16 || !(c & 1) ? 0x3f803f80u : 0u, v1 = c < 16 ? 0x00003f80u : v0;
            __builtin_amdgcn_raw_buffer_store_b128((u32x4){v0, v1, v0, v1}, rsrc, (int)WS_KV + tile * KVT_B + off, 0, AUX_WT);
        }
        for (int i = wv; i < 128 * 20; i += 192) {
            __builtin_amdgcn_raw_buffer_store_b128((u32x4){EMPTY, EMPTY, EMPTY, EMPTY}, rsrc, (int)WS_O + i * 1024 + lane * 16, 0, AUX_WT);
            __builtin_amdgcn_s_sleep(12);
        }
        return;
    }
    const int mt = (g8 & 1) * 4 + (idx & 3), cb = nq * 7 + pi;
    const int m0 = mt * 256;
    const bool mlp0 = cb < 27;
    const int t0 = mlp0 ? cb : (cb == 27 ? 27 : 29), t1 = mlp0 ? cb : (cb == 27 ? 28 : 29);
    const u16* B0 = (mlp0 ? w1 : wqkv) + t0 * 64;
    const u16* B1 = wqkv + t1 * 64;
    const int ld0 = mlp0 ? FF : 3 * DM;
    const u16* A0 = x + (size_t)m0 * DM;
    unsigned avo[4], bvo[2];
#pragma unroll
    for (int i = 0; i < 4; ++i) {
        const int row = wid * 32 + i * 8 + (lane >> 3);
        avo[i] = row * (DM * 2) + (((lane & 7) ^ ((row >> 1) & 7)) << 4);
    }
    {
        const int row = wid * 8 + (lane >> 3), ch = (lane & 7) ^ (((row >> 1) & 1) << 2);
        bvo[0] = row * ld0 * 2 + ch * 16;
        bvo[1] = row * (3 * DM * 2) + ch * 16;
    }
    const unsigned lds0 = (unsigned)(uintptr_t)smem;
    const lds_cptr lds = (lds_cptr)smem;
    const unsigned adst = lds0 + (wid >> 1) * K1_AW + (wid & 1) * 4096, bdst = lds0 + K1_BOFF + wid * 1024;
    auto dma = [&](int st, int j) {
        const int slot = st % 3;
        if (j < 4) glds16s(A0 + st * 64, avo[j], adst + slot * 8192 + j * 1024);
        else if (j == 4) glds16s(B0 + (size_t)st * 64 * ld0, bvo[0], bdst + slot * 8192);
        else glds16s(B1 + (size_t)st * 64 * (3 * DM), bvo[1], bdst + K1_AW + slot * 8192);
    };
#pragma unroll
    for (int st = 0; st < 3; ++st)
#pragma unroll
        for (int j = 0; j < 6; ++j) dma(st, j);

    const int wm = wid & 3, wn = wid >> 2, hh = lane >> 5, r = lane & 31;
    const int tq = wn ? t1 : t0;
    const bool is_mlp = mlp0 && wn == 0;
    const bool is_v = !is_mlp && tq >= 20;
    u32x2 bb[2][4];
#pragma unroll
    for (int ni = 0; ni < 2; ++ni)
#pragma unroll
        for (int gq = 0; gq < 4; ++gq) bb[ni][gq] = *(const u32x2*)(b1 + (mlp0 ? t0 * 64 : 0) + (ni ^ (wm & 1)) * 32 + 8 * gq + 4 * hh);
    f32x16 acc[2][2];
#pragma unroll
    for (int i = 0; i < 2; ++i)
#pragma unroll
        for (int j = 0; j < 2; ++j)
#pragma unroll
            for (int e = 0; e < 16; ++e) acc[i][j][e] = 0.f;
    float rs0 = 0.f, rs1 = 0.f, rq0 = 0.f, rq1 = 0.f, rs2 = 0.f, rq2 = 0.f, cs = 0.f;

    auto run = [&](auto TR) {
        constexpr bool TRANS = decltype(TR)::value;
        const int par = wm >> 1;
        lds_cptr ap[4][2];
#pragma unroll
        for (int kk = 0; kk < 4; ++kk)
#pragma unroll
            for (int mi = 0; mi < 2; ++mi)
                ap[kk][mi] = lds + wm * K1_AW + ((mi ^ wn) * 32 + r) * 128 + (((2 * (kk ^ par) + hh) ^ ((r >> 1) & 7)) << 4);
        const int g = lane >> 4, q = (lane & 15) >> 2, p = lane & 3;
        lds_cptr bp[2][2];
#pragma unroll
        for (int ni = 0; ni < 2; ++ni)
#pragma unroll
            for (int k1 = 0; k1 < 2; ++k1)
                bp[ni][k1] = lds + K1_BOFF + wn * K1_AW + (k1 ^ par) * 2048 + (8 * (g >> 1) + q) * 128 +
                             (((((ni ^ wm ^ (q >> 1)) & 1) << 2) + 2 * (g & 1) + (p >> 1)) << 4) + (p & 1) * 8;
        bf16x8 af[2][2], bfr[2][2];
        auto lda = [&](int st, int kk, int mi) { return lds128(ap[kk][mi] + (st % 3) * 8192); };
        auto ldb = [&](int st, int kk, int ni) {
            const lds_cptr s = bp[ni][kk & 1] + (st % 3) * 8192 + (kk & 2) * 2048;
            return cat8(ldstr(s), ldstr(s + 512));
        };
        auto stat = [&](unsigned u) {
            const float lo = bf_lo(u), hi = bf_hi(u);
            rs0 = vadd(rs0, lo); rq0 = vfma(lo, lo, rq0);
            rs1 = vadd(rs1, hi); rq1 = vfma(hi, hi, rq1);
        };
        WAITV_BAR(20);
        af[0][0] = lda(0, 0, 0); af[0][1] = lda(0, 0, 1); bfr[0][0] = ldb(0, 0, 0); bfr[0][1] = ldb(0, 0, 1);
        constexpr int NV = 3;
        unsigned sa[4][4], sbv[2][4];
        auto clump = [&](int first) {
#pragma unroll
            for (int k = first; k < 4; ++k)
#pragma unroll
                for (int j = NV; j < 4; ++j) { rs2 = dot2(sa[k][j], 0x3f803f80u, rs2); rq2 = dot2(sa[k][j], sa[k][j], rq2); }
#pragma unroll
            for (int k = 0; k < 2; ++k)
#pragma unroll
                for (int j = 0; j < 4; ++j) cs = dot2(sbv[k][j], 0x3f803f80u, cs);
            SB0();
        };
        static_for<40>([&](auto S) {
            constexpr int s = decltype(S)::value, st = s >> 2, kk = s & 3, c = s & 1, n = c ^ 1;
            constexpr int ns = s + 1, nst = ns >> 2, nkk = ns & 3;
            if constexpr (kk == 3) clump(st == 0 ? 1 : 0);
            if constexpr (kk == 3 && st < 9) {
                if constexpr (st == 0) { WAITV_BAR(14); } else
                if constexpr (st < 8) { WAITV_BAR(6); } else { WAITV_BAR(0); }
            }
            constexpr int ds = (s + 1) / 4 + 2, dj = (s + 1) & 3;
            constexpr bool dm = s >= 3 && ds < 10;
            const u32x4 a = __builtin_bit_cast(u32x4, af[c][0]);
#pragma unroll
            for (int j = 0; j < 4; ++j) sa[(kk + 1) & 3][j] = a[j];
            if constexpr ((kk & 1) == 0) {
                const u32x4 b = __builtin_bit_cast(u32x4, bfr[c][0]);
#pragma unroll
                for (int j = 0; j < 4; ++j) sbv[kk >> 1][j] = b[j];
            }
#define K1_MM(mi, ni) acc[mi][ni] = TRANS ? MFMA32(bfr[c][ni], af[c][mi], acc[mi][ni]) : MFMA32(af[c][mi], bfr[c][ni], acc[mi][ni])
            K1_MM(0, 0);
            if constexpr (ns < 40) af[n][0] = lda(nst, nkk, 0);
            if constexpr (NV >= 1) stat(a[0]);
            SB0();
            K1_MM(0, 1);
            if constexpr (ns < 40) bfr[n][0] = ldb(nst, nkk, 0);
            if constexpr (dm) dma(ds, dj < 2 ? 2 * dj : dj + 2);
            if constexpr (NV >= 4) stat(a[3]);
            SB0();
            K1_MM(1, 0);
            if constexpr (ns < 40) bfr[n][1] = ldb(nst, nkk, 1);
            if constexpr (NV >= 2) stat(a[1]);
            SB0();
            K1_MM(1, 1);
            if constexpr (ns < 40) af[n][1] = lda(nst, nkk, 1);
            if constexpr (dm && dj < 2) dma(ds, 2 * dj + 1);
            if constexpr (NV >= 3) stat(a[2]);
            SB0();
        });
#pragma unroll
        for (int j = NV; j < 4; ++j) { rs2 = dot2(sa[0][j], 0x3f803f80u, rs2); rq2 = dot2(sa[0][j], sa[0][j], rq2); }
    };
    if (is_v) run(std::integral_constant<bool, false>{}); else run(std::integral_constant<bool, true>{});

    float* st_mu = (float*)(smem + 6 * K1_AW);
    float* st_r1 = st_mu + 256;
    float* st_r2 = st_r1 + 256;
    float* st_cs = st_r2 + 256;
    float rs = rs0 + rs1 + rs2, rq = rq0 + rq1 + rq2;
    rs += xhalf(rs); rq += xhalf(rq); cs += xhalf(cs);
    {
        const float mu = rs * (1.0f / DM);
        const float var = __builtin_fmaxf(rq * (1.0f / DM) - mu * mu, 0.f);
        const float r1 = __builtin_amdgcn_rsqf(var + LN_EPS);
        const float r12 = r1 * __builtin_amdgcn_rsqf(var * r1 * r1 + LN_EPS);
        float* pa = hh ? st_r2 + wm * 64 + wn * 32 + r : st_mu + wm * 64 + wn * 32 + r;
        float* pb = hh ? st_cs + (wm >> 1) * 128 + wn * 64 + (wm & 1) * 32 + r : st_r1 + wm * 64 + wn * 32 + r;
        *pa = hh ? r12 : mu;
        *pb = hh ? cs : r1;
    }
    BAR_L();
    if (cb == 28 && wn == 1) return;
    if (!is_mlp) __builtin_amdgcn_s_setprio(3);
    const int n0 = tq * 64;

    if (is_v) {
        f32x4 mu[2][4], sc[2][4];
#pragma unroll
        for (int mi = 0; mi < 2; ++mi)
#pragma unroll
            for (int q4 = 0; q4 < 4; ++q4) {
                mu[mi][q4] = *(const f32x4*)(st_mu + wm * 64 + (mi ^ wn) * 32 + 8 * q4 + 4 * hh);
                sc[mi][q4] = *(const f32x4*)(st_r1 + wm * 64 + (mi ^ wn) * 32 + 8 * q4 + 4 * hh);
            }
#pragma unroll
        for (int ni = 0; ni < 2; ++ni) {
            const int cl = (ni ^ (wm & 1)) * 32 + r;
            const float c = st_cs[wn * 64 + cl] + st_cs[128 + wn * 64 + cl];
            const int nn = n0 - 2 * DM + cl, head = nn / DH, d = nn - head * DH;
#pragma unroll
            for (int mi = 0; mi < 2; ++mi) {
                const int rl = wm * 64 + (mi ^ wn) * 32;
                const int dst = (int)WS_KV + (head * 64 + ((m0 + rl) >> 5)) * KVT_B + 1536 + (hh * 24 + d) * 16;
#pragma unroll
                for (int s = 0; s < 2; ++s) {
                    u32x4 o;
#pragma unroll
                    for (int gq = 0; gq < 2; ++gq) {
                        const f32x4 a = {acc[mi][ni][8 * s + 4 * gq], acc[mi][ni][8 * s + 4 * gq + 1], acc[mi][ni][8 * s + 4 * gq + 2], acc[mi][ni][8 * s + 4 * gq + 3]};
                        const f32x4 v = (a - mu[mi][2 * s + gq] * c) * sc[mi][2 * s + gq];
                        o[2 * gq] = cvtpk(v[0], v[1]); o[2 * gq + 1] = cvtpk(v[2], v[3]);
                    }
                    __builtin_amdgcn_raw_buffer_store_b128(o, rsrc, dst + s * 768, 0, AUX_WT);
                }
            }
        }
    } else {
        const bool isk = tq >= 10;
        float sc[2], cm[2];
#pragma unroll
        for (int mi = 0; mi < 2; ++mi) {
            const int rl = wm * 64 + (mi ^ wn) * 32 + r;
            sc[mi] = is_mlp ? st_r2[rl] : st_r1[rl] * (isk ? 1.0f : QSCALE);
            cm[mi] = -st_mu[rl] * sc[mi];
        }
        f32x4 cc[2][4];
#pragma unroll
        for (int ni = 0; ni < 2; ++ni)
#pragma unroll
            for (int gq = 0; gq < 4; ++gq) {
                const int cl = wn * 64 + (ni ^ (wm & 1)) * 32 + 8 * gq + 4 * hh;
                cc[ni][gq] = *(const f32x4*)(st_cs + cl) + *(const f32x4*)(st_cs + 128 + cl);
            }
        if (is_mlp) {
            float c1 = -2.0f * 0.7978845608028654f * LOG2E, c2 = c1 * 0.044715f;
            asm volatile("" : "+v"(c1), "+v"(c2));
#pragma unroll
            for (int ni = 0; ni < 2; ++ni) {
#pragma unroll
                for (int mi = 0; mi < 2; ++mi) {
                    unsigned pk[8];
#pragma unroll
                    for (int gq = 0; gq < 4; ++gq) {
                        const f32x4 bv = {bf_lo(bb[ni][gq][0]), bf_hi(bb[ni][gq][0]), bf_lo(bb[ni][gq][1]), bf_hi(bb[ni][gq][1])};
                        const f32x4 a = {acc[mi][ni][4 * gq], acc[mi][ni][4 * gq + 1], acc[mi][ni][4 * gq + 2], acc[mi][ni][4 * gq + 3]};
                        const f32x4 u = a * sc[mi] + (cc[ni][gq] * cm[mi] + bv);
                        const f32x4 t = u * (u * u * c2 + c1);
                        f32x4 d;
#pragma unroll
                        for (int e = 0; e < 4; ++e) d[e] = __builtin_amdgcn_exp2f(t[e]);
                        d = d + 1.0f;
#pragma unroll
                        for (int e = 0; e < 4; ++e) d[e] = __builtin_amdgcn_rcpf(d[e]);
                        const f32x4 v = u * d;
                        pk[2 * gq] = cvtpk(v[0], v[1]); pk[2 * gq + 1] = cvtpk(v[2], v[3]);
                    }
                    const int t16 = (m0 + wm * 64 + mi * 32 + r) >> 4;
                    const int dst = (int)WS_H + (t16 * 54 + ((n0 + (ni ^ (wm & 1)) * 32) >> 5)) * 1024 + (hh * 32 + (r & 15)) * 16;
                    __builtin_amdgcn_raw_buffer_store_b128((u32x4){pk[0], pk[1], pk[2], pk[3]}, rsrc, dst, 0, AUX_WT);
                    __builtin_amdgcn_raw_buffer_store_b128((u32x4){pk[4], pk[5], pk[6], pk[7]}, rsrc, dst + 256, 0, AUX_WT);
                }
            }
        } else {
            const int tb = isk ? KVT_B : QT_B;
            const int base = (int)(isk ? WS_KV : WS_Q) + ((m0 + wm * 64) >> 5) * tb;
#pragma unroll
            for (int ni = 0; ni < 2; ++ni)
#pragma unroll
                for (int gq = 0; gq < 4; ++gq) {
                    const int P = ((n0 - (isk ? DM : 0) + (ni ^ (wm & 1)) * 32) >> 2) + 2 * gq + hh;
                    const int head = P / 5, pc = P - head * 5;
                    const bool odd = r & 1;
                    const int off = base + head * 64 * tb + (pc < 4 ? (pc & 1) * 512 + (pc >> 1) * 256 : 1024) + (r & ~1) * 8 + ((r & 1) ^ wn) * tb;
                    unsigned w[2][2];
#pragma unroll
                    for (int mi = 0; mi < 2; ++mi) {
                        const f32x4 a = {acc[mi][ni][4 * gq], acc[mi][ni][4 * gq + 1], acc[mi][ni][4 * gq + 2], acc[mi][ni][4 * gq + 3]};
                        const f32x4 v = a * sc[mi] + cc[ni][gq] * cm[mi];
                        w[mi][0] = cvtpk(v[0], v[1]); w[mi][1] = cvtpk(v[2], v[3]);
                    }
                    const unsigned r0 = __builtin_amdgcn_mov_dpp(odd ? w[0][0] : w[1][0], 0xB1, 0xf, 0xf, true);
                    const unsigned r1 = __builtin_amdgcn_mov_dpp(odd ? w[0][1] : w[1][1], 0xB1, 0xf, 0xf, true);
                    __builtin_amdgcn_raw_buffer_store_b128((u32x4){odd ? r0 : w[0][0], odd ? r1 : w[0][1], odd ? w[1][0] : r0, odd ? w[1][1] : r1},
                                                           rsrc, off, 0, AUX_WT);
                }
        }
    }
}

constexpr int AT_STG = 8 * KVT_B, K2_LDS = 8 * 20 * 1024;
constexpr int GREG = 4 * 1280;
typedef __attribute__((address_space(1))) unsigned gu32;
#define BARRIER() asm volatile("s_barrier" ::: "memory")
DI void mfma16(f32x4& c, bf16x8 a, bf16x8 b, bool first) {
    if (first) asm volatile("v_mfma_f32_16x16x32_bf16 %0, %1, %2, 0" : "=&v"(c) : "v"(a), "v"(b));
    else asm volatile("v_mfma_f32_16x16x32_bf16 %0, %1, %2, %0" : "+v"(c) : "v"(a), "v"(b));
}

__global__ __launch_bounds__(768) void k2_attn(const u16* __restrict__ x, const u16* __restrict__ b2, char* __restrict__ ws,
                                               u16* __restrict__ out) {
    extern __shared__ __attribute__((aligned(16))) char smem[];
    const int tid = threadIdx.x, lane = tid & 63, wid = __builtin_amdgcn_readfirstlane(tid >> 6);
    const int hh = lane >> 5, r = lane & 31;
    const int g8 = blockIdx.x & 7, idx = blockIdx.x >> 3;
    const int head = g8 * 4 + (idx >> 3), qt = (idx & 7) * 8 + wid;
    const int tb = g8 * 4 + (idx >> 3), nb = idx & 7;
    const unsigned lds0 = (unsigned)(uintptr_t)smem;
    const lds_cptr lds = (lds_cptr)smem;
    const unsigned l16 = lane * 16;
    const auto rsrc = __builtin_amdgcn_make_buffer_rsrc(ws, 0, (int)WS_END, 0x00020000);
    f32x4* red = (f32x4*)smem;
    auto rpos = [&](int reg, int i, int u) { return (reg * 20 + i * 5 + u) * 64 + (lane ^ (2 * u + hh)); };
    auto cpos = [&](int row, int ch) { return ((row >> 4) * 5 + (ch >> 1)) * 64 + (ch & 1) * 32 + ((row & 15) ^ ch); };
    if (wid >= 8) {
        const int gw = wid - 8, k0 = gw < 2 ? gw * 14 : gw * 13 + 2;
        const int abase = (int)WS_H + (tb * 4 * 54 + k0) * 1024, wbase = (int)WS_W2F + (nb * 5 * 54 + k0) * 1024;
        const auto rs_t = __builtin_amdgcn_make_buffer_rsrc(ws, 0, gw < 2 ? (int)WS_END : 0, 0x00020000);
        bf16x8 af[2][4], wf[2][5];
        f32x4 acc[4][5];
        auto ld = [&](int j) {
#pragma unroll
            for (int u = 0; u < 5; ++u) {
                wf[j & 1][u] = __builtin_bit_cast(bf16x8, __builtin_amdgcn_raw_buffer_load_b128(j == 13 ? rs_t : rsrc, l16, wbase + (u * 54 + j) * 1024, 0));
                if (u < 4) af[j & 1][u] = __builtin_bit_cast(bf16x8, __builtin_amdgcn_raw_buffer_load_b128(j == 13 ? rs_t : rsrc, l16, abase + (u * 54 + j) * 1024, 0));
            }
            SB0();
        };
        ld(0);
        u32x2 bb[5];
#pragma unroll
        for (int u = 0; u < 5; ++u) bb[u] = *(const u32x2*)(b2 + nb * 80 + u * 16 + (lane >> 4) * 4);
#pragma unroll
        for (int j = 0; j < 14; ++j) {
            if ((j & 1) == 0) BARRIER();
            if (j == 12) { BARRIER(); BARRIER(); }
            if (j == 0) {
                f32x4 bv[5];
#pragma unroll
                for (int u = 0; u < 5; ++u) {
                    if (gw) bb[u] = (u32x2){0u, 0u};
                    bv[u] = (f32x4){bf_lo(bb[u][0]), bf_hi(bb[u][0]), bf_lo(bb[u][1]), bf_hi(bb[u][1])};
                }
                asm volatile("s_nop 3" : "+v"(bv[0]), "+v"(bv[1]), "+v"(bv[2]), "+v"(bv[3]), "+v"(bv[4]));
#pragma unroll
                for (int i = 0; i < 4; ++i)
#pragma unroll
                    for (int u = 0; u < 5; ++u)
                        asm volatile("v_mfma_f32_16x16x32_bf16 %0, %1, %2, %3" : "=&v"(acc[i][u]) : "v"(wf[0][u]), "v"(af[0][i]), "v"(bv[u]));
            } else {
#pragma unroll
                for (int i = 0; i < 4; ++i)
#pragma unroll
                    for (int u = 0; u < 5; ++u) mfma16(acc[i][u], wf[j & 1][u], af[j & 1][i], false);
            }
            SB0();
            if (j == 0) ld(1);
            if (j + 2 < 14) ld(j + 2);
        }
        asm volatile("s_nop 15\n\ts_nop 15");
        SB0();
#pragma unroll
        for (int i = 0; i < 4; ++i)
#pragma unroll
            for (int u = 0; u < 5; ++u) red[GREG + rpos(gw, i, u)] = acc[i][u];
        BAR_L();
        f32x4* src = red + GREG + gw * 64 + lane;
        f32x4 pt[5][4];
#pragma unroll
        for (int c = 0; c < 5; ++c)
#pragma unroll
            for (int w = 0; w < 4; ++w) pt[c][w] = src[c * 256 + w * 1280];
        SB0();
#pragma unroll
        for (int c = 0; c < 5; ++c) {
            f32x4 g = (pt[c][0] + pt[c][1]) + (pt[c][2] + pt[c][3]);
#pragma unroll
            for (int e = 0; e < 4; ++e) g[e] = g[e] * __builtin_amdgcn_rcpf(1.0f + __builtin_amdgcn_exp2f(-LOG2E * g[e]));
            src[c * 256 + 1280] = g;
        }
        BAR_L();
        return;
    }
    {
    __builtin_amdgcn_s_setprio(2);
    const char* kvsrc = ws + WS_KV + (size_t)(head * 64 + wid) * KVT_B;
    auto dma = [&](int st, int i) { glds16s(kvsrc + (size_t)st * AT_STG + i * 1024, l16, lds0 + (st % 3) * AT_STG + wid * KVT_B + i * 1024); };
    const char* qsrc = ws + WS_Q + (size_t)(head * 64 + qt) * QT_B;
    u32x2 q1a, q1b, q2u;
    asm volatile("global_load_dwordx2 %0, %1, %2" : "=v"(q1a) : "v"(lane * 8), "s"(qsrc) : "memory");
    asm volatile("global_load_dwordx2 %0, %1, %2 offset:512" : "=v"(q1b) : "v"(lane * 8), "s"(qsrc) : "memory");
    asm volatile("global_load_dwordx2 %0, %1, %2 offset:1024" : "=v"(q2u) : "v"(r * 8), "s"(qsrc) : "memory");
#pragma unroll
    for (int st = 0; st < 3; ++st)
#pragma unroll
        for (int i = 0; i < 3; ++i) dma(st, i);

    const int mh = wid & 1, kq = wid >> 1;
    const int abase = (int)WS_O + ((tb * 4 + mh * 2) * 20 + kq * 5) * 1024, wbase = (int)WS_WOF + (nb * 5 * 20 + kq * 5) * 1024;
    bf16x8 af[5][2], wf[4][5];
    f32x4 acc[2][5];
    u32x4 xx[2];
    auto ldw1 = [&](int j, int u) {
        wf[j & 3][u] = __builtin_bit_cast(bf16x8, __builtin_amdgcn_raw_buffer_load_b128(rsrc, l16, wbase + (u * 20 + j) * 1024, 0));
    };
    auto ldw = [&](int j) {
#pragma unroll
        for (int u = 0; u < 5; ++u) ldw1(j, u);
    };
    const int reff = r < 21 ? r : 21;
    const lds_cptr k1p = lds + lane * 8, k2p = lds + 1024 + lane * 8, vp = lds + 1536 + (hh * 24 + reff) * 16;
    auto toff = [](int T) { return ((T >> 3) % 3) * AT_STG + (T & 7) * KVT_B; };
    auto ldk1 = [&](int T) { return cat8(lds64(k1p + toff(T)), lds64(k1p + toff(T) + 512)); };
    f32x16 zero16;
#pragma unroll
    for (int e = 0; e < 16; ++e) zero16[e] = 0.f;
    f32x16 o = zero16;

    auto set_m = [&](float m) {
        const unsigned a = cvtpk(-m, 0.f) & 0xffffu;
        const float r1 = -m - bf_lo(a);
        const unsigned b = cvtpk(r1, 0.f) & 0xffffu;
        const float r2 = r1 - bf_lo(b);
        const unsigned c = cvtpk(r2, 0.f) & 0xffffu;
        if (hh) q2u = (u32x2){a | (b << 16), c};
    };

    asm volatile("s_waitcnt vmcnt(6)\n\ts_barrier" : "+v"(q1a), "+v"(q1b), "+v"(q2u) :: "memory");
    if (hh) q2u = (u32x2){0u, 0u};
    const bf16x8 q1 = __builtin_bit_cast(bf16x8, (u32x4){q1a[0], q1a[1], q1b[0], q1b[1]});
    {
        f32x16 s = MFMA32(ldk1(0), q1, zero16);
        s = MFMA32K8(lds64(k2p), __builtin_bit_cast(bf16x4, q2u), s);
        float m = s[0];
#pragma unroll
        for (int e = 1; e < 16; ++e) m = __builtin_fmaxf(m, s[e]);
        m = __builtin_fmaxf(m, xhalf(m));
        set_m(m);
    }
    {
        const bf16x4 q2 = __builtin_bit_cast(bf16x4, q2u);
        f32x16 sb[2];
        sb[0] = MFMA32(ldk1(0), q1, zero16);
        sb[0] = MFMA32K8(lds64(k2p), q2, sb[0]);
        bf16x8 k1 = ldk1(1), v0 = lds128(vp), v1;
        bf16x4 k2 = lds64(k2p + toff(1));
        u32x4 p0, p1;
        SB0();
        static_for<256>([&](auto GI) {
            constexpr int g = decltype(GI)::value, T = g >> 2, sl = g & 3, st = T >> 3;
            constexpr int c = T & 1, n = c ^ 1;
            if constexpr ((T & 7) == 6 && sl == 0 && st < 7) {
                if constexpr (st == 0) asm volatile("s_waitcnt vmcnt(3)\n\ts_barrier" ::: "memory");
                else asm volatile("s_waitcnt vmcnt(0)\n\ts_barrier" ::: "memory");
            }
            if constexpr (sl == 0) {
                if constexpr (T < 63) sb[n] = MFMA32(k1, q1, zero16);
                if constexpr (T < 62) k1 = ldk1(T + 2);
            } else if constexpr (sl == 1) {
                if constexpr (T > 0) o = MFMA32(v1, __builtin_bit_cast(bf16x8, p1), o);
                v1 = lds128(vp + toff(T) + 768);
            } else if constexpr (sl == 2) {
                if constexpr (T < 63) sb[n] = MFMA32K8(k2, q2, sb[n]);
                if constexpr (T < 62) k2 = lds64(k2p + toff(T + 2));
            } else {
                o = MFMA32(v0, __builtin_bit_cast(bf16x8, p0), o);
                if constexpr (T < 63) v0 = lds128(vp + toff(T + 1));
            }
            if constexpr ((T & 7) == 6 && sl < 3 && st >= 1 && st + 2 <= 7) dma(st + 2, sl);
            if constexpr (g >= 218 && g < 218 + 5 * 3) ldw1((g - 218) / 5, (g - 218) % 5);
            SB0();
            const float e0 = __builtin_amdgcn_exp2f(sb[c][4 * sl]), e1 = __builtin_amdgcn_exp2f(sb[c][4 * sl + 1]);
            const float e2 = __builtin_amdgcn_exp2f(sb[c][4 * sl + 2]), e3 = __builtin_amdgcn_exp2f(sb[c][4 * sl + 3]);
            if constexpr (sl < 2) { p0[2 * sl] = cvtpk(e0, e1); p0[2 * sl + 1] = cvtpk(e2, e3); }
            else { p1[2 * sl - 4] = cvtpk(e0, e1); p1[2 * sl - 3] = cvtpk(e2, e3); }
            SB0();
        });
        o = MFMA32(v1, __builtin_bit_cast(bf16x8, p1), o);
    }
    float l = o[8];
    { const float lx = xhalf(l); l = hh ? l : lx; }
    bool bad = !(l < 1.0e18f) || !(l > 0.25f);
#pragma unroll
    for (int e = 0; e < 12; ++e) bad |= !(__builtin_fabsf(o[e]) < 3.0e38f) && (e < 8 || !hh);
    if (__builtin_amdgcn_ballot_w64(bad) != 0) {
        const char* kvh = ws + WS_KV + (size_t)head * 64 * KVT_B;
        for (int pass = 0; pass < 2; ++pass) {
            if (pass == 0 && hh) q2u = (u32x2){0u, 0u};
            float m = -INFINITY;
            o = zero16;
            for (int T = 0; T < 64; ++T) {
                const char* kt = kvh + T * KVT_B;
                f32x16 s = MFMA32(cat8(*(const bf16x4*)(kt + lane * 8), *(const bf16x4*)(kt + 512 + lane * 8)), q1, zero16);
                s = MFMA32K8(*(const bf16x4*)(kt + 1024 + lane * 8), __builtin_bit_cast(bf16x4, q2u), s);
                if (pass == 0) {
#pragma unroll
                    for (int e = 0; e < 16; ++e) m = __builtin_fmaxf(m, s[e]);
                } else {
                    u32x4 p0, p1;
#pragma unroll
                    for (int e = 0; e < 4; ++e) {
                        p0[e] = cvtpk(__builtin_amdgcn_exp2f(s[2 * e]), __builtin_amdgcn_exp2f(s[2 * e + 1]));
                        p1[e] = cvtpk(__builtin_amdgcn_exp2f(s[8 + 2 * e]), __builtin_amdgcn_exp2f(s[9 + 2 * e]));
                    }
                    const char* vt = kt + 1536 + (hh * 24 + reff) * 16;
                    o = MFMA32(*(const bf16x8*)vt, __builtin_bit_cast(bf16x8, p0), o);
                    o = MFMA32(*(const bf16x8*)(vt + 768), __builtin_bit_cast(bf16x8, p1), o);
                }
            }
            if (pass == 0) { m = __builtin_fmaxf(m, xhalf(m)); set_m(m); }
        }
        l = o[8];
        { const float lx = xhalf(l); l = hh ? l : lx; }
    }
    const float il = 1.0f / l;
    auto pk = [&](int e) { return __builtin_elementwise_min(cvtpk(o[e] * il, o[e + 1] * il), EMPTY - 1u); };
    const int obase = (int)WS_O + (qt * 2 + (r >> 4)) * 20 * 1024 + (r & 15) * 16;
#pragma unroll
    for (int i = 0; i < 3; ++i) {
        if (i < 2 || hh == 0) {
            const int G = 5 * head + 2 * i + hh;
            __builtin_amdgcn_raw_buffer_store_b64((u32x2){pk(4 * i), pk(4 * i + 2)}, rsrc,
                                                  obase + (G >> 3) * 1024 + ((G >> 1) & 3) * 256 + (G & 1) * 8, 0, AUX_WT);
        }
    }
    BAR_L();
#pragma unroll
    for (int j = 3; j < 4; ++j) ldw(j);
    SB0();
    {
        const int pg = 5 * (kq * 8 + (lane & 7)) + 4;
        gu32* probe = (gu32*)(ws + WS_O + (unsigned)(((tb * 4 + mh * 2 + ((lane >> 3) & 1)) * 20 + (pg >> 3)) * 1024 + ((pg >> 1) & 3) * 256 + (pg & 1) * 8 + 15 * 16 + 4));
#pragma unroll 1
        for (unsigned spin = 0; spin < (1u << 22); ++spin)
            if (__builtin_amdgcn_ballot_w64(__hip_atomic_load(probe, __ATOMIC_RELAXED, __HIP_MEMORY_SCOPE_AGENT) == EMPTY) == 0) break;
    }
    __builtin_amdgcn_fence(__ATOMIC_ACQUIRE, "wavefront");
    auto lda = [&](int j, int i) {
        return __builtin_bit_cast(bf16x8, __builtin_amdgcn_raw_buffer_load_b128(rsrc, l16, abase + (i * 20 + j) * 1024, AUX_WT));
    };
    auto umax = [](unsigned m, bf16x8 v) {
        const u32x4 a = __builtin_bit_cast(u32x4, v);
        m = __builtin_elementwise_max(__builtin_elementwise_max(m, a[0]), a[1]);
        return __builtin_elementwise_max(__builtin_elementwise_max(m, a[2]), a[3]);
    };
#pragma unroll
    for (int j = 0; j < 5; ++j)
#pragma unroll
        for (int i = 0; i < 2; ++i) af[j][i] = lda(j, i);
    SB0();
    BARRIER();
    SB0();
    unsigned mx = 0;
#pragma unroll
    for (int j = 0; j < 5; ++j) {
#pragma unroll
        for (int i = 0; i < 2; ++i)
#pragma unroll
            for (int u = 0; u < 5; ++u) mfma16(acc[i][u], wf[j & 3][u], af[j][i], j == 0);
        SB0();
        mx = umax(umax(mx, af[j][0]), af[j][1]);
        SB0();
        if (j == 0) { ldw(4); SB0(); }
        if (j == 1) {
#pragma unroll
            for (int ps = 0; ps < 2; ++ps) {
                const int c = (wid < 2 ? ps * 512 : 0) + wid * 64 + lane_id(), row = c / 10, ch = c - row * 10;
                xx[ps] = *(const u32x4*)(x + (size_t)(tb * 64 + row) * DM + nb * 80 + ch * 8);
            }
            SB0();
        }
    }
    if (__builtin_amdgcn_ballot_w64(mx == EMPTY) != 0) {
        asm volatile("s_nop 15\n\ts_nop 15");
        SB0();
#pragma unroll
        for (int i = 0; i < 2; ++i)
#pragma unroll
            for (int u = 0; u < 5; ++u) acc[i][u] = (f32x4){0.f, 0.f, 0.f, 0.f};
#pragma unroll 1
        for (int j = 0; j < 5; ++j) {
            bf16x8 a0, a1;
#pragma unroll 1
            for (unsigned spin = 0;; ++spin) {
                asm volatile("" ::: "memory");
                a0 = lda(j, 0); a1 = lda(j, 1);
                if (__builtin_amdgcn_ballot_w64(umax(umax(0u, a0), a1) == EMPTY) == 0 || spin >= (1u << 16)) break;
                __builtin_amdgcn_s_sleep(2);
            }
#pragma unroll
            for (int u = 0; u < 5; ++u) {
                const bf16x8 w = __builtin_bit_cast(bf16x8, __builtin_amdgcn_raw_buffer_load_b128(rsrc, l16, wbase + (u * 20 + j) * 1024, 0));
                acc[0][u] = __builtin_amdgcn_mfma_f32_16x16x32_bf16(w, a0, acc[0][u], 0, 0, 0);
                acc[1][u] = __builtin_amdgcn_mfma_f32_16x16x32_bf16(w, a1, acc[1][u], 0, 0, 0);
            }
        }
    }
    asm volatile("s_nop 15\n\ts_nop 15");
    SB0();
#pragma unroll
    for (int i = 0; i < 2; ++i)
#pragma unroll
        for (int u = 0; u < 5; ++u) red[rpos(kq, mh * 2 + i, u)] = acc[i][u];
    BAR_L();
#pragma unroll
    for (int ps = 0; ps < 2; ++ps) {
        if (ps == 0 || wid < 2) {
            const int c = ps * 512 + tid, row = c / 10, ch = c - row * 10;
            const f32x4* src = red + cpos(row, ch);
            const f32x4 s0 = (src[0] + src[1280]) + (src[2560] + src[3840]), s1 = (src[16] + src[1296]) + (src[2576] + src[3856]);
            const f32x4 g0 = src[GREG + 1280], g1 = src[GREG + 1296];
            const u32x4 xv = xx[ps];
            u32x4 ov;
            ov[0] = cvtpk(__builtin_fmaf(s0[0], g0[0], bf_lo(xv[0])), __builtin_fmaf(s0[1], g0[1], bf_hi(xv[0])));
            ov[1] = cvtpk(__builtin_fmaf(s0[2], g0[2], bf_lo(xv[1])), __builtin_fmaf(s0[3], g0[3], bf_hi(xv[1])));
            ov[2] = cvtpk(__builtin_fmaf(s1[0], g1[0], bf_lo(xv[2])), __builtin_fmaf(s1[1], g1[1], bf_hi(xv[2])));
            ov[3] = cvtpk(__builtin_fmaf(s1[2], g1[2], bf_lo(xv[3])), __builtin_fmaf(s1[3], g1[3], bf_hi(xv[3])));
            *(u32x4*)(out + (size_t)(tb * 64 + row) * DM + nb * 80 + ch * 8) = ov;
        }
    }
    }
}

extern "C" void kernel_launch(void* const* d_in, const int* in_sizes, int n_in, void* d_out, int out_size, void* d_ws, size_t ws_size,
                              hipStream_t stream) {
    const u16* x = (const u16*)d_in[0];
    const u16* wqkv = (const u16*)d_in[1];
    const u16* wo = (const u16*)d_in[2];
    const u16* w1 = (const u16*)d_in[4];
    const u16* b1 = (const u16*)d_in[5];
    const u16* w2 = (const u16*)d_in[6];
    const u16* b2 = (const u16*)d_in[7];
    char* ws = (char*)d_ws;
    (void)hipFuncSetAttribute((const void*)k1_proj, hipFuncAttributeMaxDynamicSharedMemorySize, K1_LDS);
    (void)hipFuncSetAttribute((const void*)k2_attn, hipFuncAttributeMaxDynamicSharedMemorySize, K2_LDS);
    k1_proj<<<256, 512, K1_LDS, stream>>>(x, wqkv, w1, b1, w2, wo, ws);
    k2_attn<<<256, 768, K2_LDS, stream>>>(x, b2, ws, (u16*)d_out);
}
```
